```python
import torch

def _softmax(x, axis=-1):
    return torch.softmax(x, dim=axis)

def _gelu(x):
    return torch.nn.functional.gelu(x, approximate='tanh')

def _silu(x):
    return torch.nn.functional.silu(x)

def _sigmoid(x):
    return torch.sigmoid(x)

def _layernorm(x, axis=-1, eps=1e-05):
    m = x.mean(dim=axis, keepdim=True)
    v = x.var(dim=axis, keepdim=True, unbiased=False)
    return (x - m) / torch.sqrt(v + eps)

def _scatter_add(tgt, idx, src, axis=0):
    return tgt.clone().index_add_(axis, idx, src)

def _segment_sum(src, seg, n):
    out = torch.zeros((n,) + src.shape[1:], dtype=src.dtype)
    return out.index_add_(0, seg, src)

def _pad(x, pw):
    flat = tuple((v for pair in reversed(pw) for v in pair))
    return torch.nn.functional.pad(x, flat)
kernel_name = 'parallel_sum__memory_aug_recurrent_mem_tr__glu_gated_bilinear__prompt'

def setup_inputs():

    _w = lambda a: (lambda t: t.to(torch.bfloat16) if t.is_floating_point() else t)(torch.as_tensor(a))

    def _A_setup():
        B, S, Dm = (1, 2048, 448)
        H, Dh = (16, 256)
        M, seg = (8, 32)
        seg = min(seg, S)
        S = S // seg * seg
        return {'x': _w((lambda *s: torch.randn(*s, dtype=torch.bfloat16) * 0.05)(B, S, Dm)), 'mem0': _w((lambda *s: torch.randn(*s, dtype=torch.bfloat16) * 0.2)(B, M, Dm)), 'Wq': _w((lambda *s: torch.randn(*s, dtype=torch.bfloat16) * 1.6)(Dm, H, Dh) * Dm ** (-0.5)), 'Wk': _w((lambda *s: torch.randn(*s, dtype=torch.bfloat16) * 1.6)(Dm, H, Dh) * Dm ** (-0.5)), 'Wv': _w((lambda *s: torch.randn(*s, dtype=torch.bfloat16) * 0.2)(Dm, H, Dh) * Dm ** (-0.5)), 'Wo': _w((lambda *s: torch.randn(*s, dtype=torch.bfloat16) * 0.2)(H, Dh, Dm) * (H * Dh) ** (-0.5)), 'seg_len': seg}
    _A_in = _A_setup()

    def _B_setup():
        B, S, D, F = (1, 2048, 448, 1792)
        return {'x': _w((lambda *s: torch.randn(*s, dtype=torch.bfloat16) * 0.05)(B, S, D)), 'w_gate': _w((lambda *s: torch.randn(*s, dtype=torch.bfloat16) * 0.2)(D, F) * D ** (-0.5)), 'w_up': _w((lambda *s: torch.randn(*s, dtype=torch.bfloat16) * 0.2)(D, F) * D ** (-0.5)), 'w_down': _w((lambda *s: torch.randn(*s, dtype=torch.bfloat16) * 0.2)(F, D) * F ** (-0.5))}
    _B_in = _B_setup()
    return {'A_x': _A_in['x'], 'A_mem0': _A_in['mem0'], 'A_Wq': _A_in['Wq'], 'A_Wk': _A_in['Wk'], 'A_Wv': _A_in['Wv'], 'A_Wo': _A_in['Wo'], 'A_seg_len': _A_in['seg_len'], 'B_x': _B_in['x'], 'B_w_gate': _B_in['w_gate'], 'B_w_up': _B_in['w_up'], 'B_w_down': _B_in['w_down']}

def reference(A_x, A_mem0, A_Wq, A_Wk, A_Wv, A_Wo, A_seg_len, B_x, B_w_gate, B_w_up, B_w_down):

    def _A_ref(x, mem0, Wq, Wk, Wv, Wo, seg_len):
        B, S, Dm = x.shape
        H, Dh = (Wq.shape[1], Wq.shape[2])
        M = mem0.shape[1]
        scale = Dh ** (-0.5)
        n_seg = S // seg_len
        L = M + seg_len
        idx = torch.arange(L)
        mask = (idx[:, None] < M) | (idx[:, None] >= idx[None, :])
        mem = mem0
        outs = []
        for s in range(n_seg):
            xs = x[:, s * seg_len:(s + 1) * seg_len, :].clone()
            h = torch.concatenate([mem, xs], axis=1)
            q = torch.einsum('btd,dhe->bhte', h, Wq)
            k = torch.einsum('btd,dhe->bhte', h, Wk)
            v = torch.einsum('btd,dhe->bhte', h, Wv)
            scores = torch.einsum('bhqe,bhke->bhqk', q, k) * scale
            scores = torch.where(mask[None, None], scores, -1000000000.0)
            p = _softmax(scores, axis=-1)
            o = torch.einsum('bhqk,bhke->bhqe', p, v)
            o = torch.einsum('bhte,hed->btd', o, Wo)
            mem = o[:, :M, :].clone()
            outs.append(o[:, M:, :])
        return torch.concatenate(outs, axis=1)

    def _B_ref(x, w_gate, w_up, w_down):
        g = torch.einsum('bsd,df->bsf', x, w_gate)
        u = torch.einsum('bsd,df->bsf', x, w_up)
        h = g * u
        return torch.einsum('bsf,fd->bsd', h, w_down)
    x = A_x
    xn = _layernorm(x, axis=-1)
    return x + _A_ref(xn, A_mem0, A_Wq, A_Wk, A_Wv, A_Wo, A_seg_len) + _B_ref(xn, B_w_gate, B_w_up, B_w_down)
```

```cpp
#include <hip/hip_runtime.h>
#include <hip/hip_bf16.h>

typedef unsigned v4u __attribute__((ext_vector_type(4)));
typedef unsigned v2u __attribute__((ext_vector_type(2)));
typedef float f32x4 __attribute__((ext_vector_type(4)));
typedef float f32x2 __attribute__((ext_vector_type(2)));
typedef __bf16 bf16x8 __attribute__((ext_vector_type(8)));
typedef __bf16 bf16x2 __attribute__((ext_vector_type(2)));
typedef __amdgpu_buffer_rsrc_t rsrc_t;
#define LAS __attribute__((address_space(3)))
#define GAS __attribute__((address_space(1)))
#define DEV __device__ __forceinline__

constexpr int S_ = 2048, DM = 448, NH = 16, DH = 256, FF = 1792, NSEG = 64;
constexpr unsigned MB = 1u << 20;
constexpr unsigned OFF_HO = 0;
constexpr unsigned OFF_CTL = 4 * MB;
constexpr unsigned CTL_KV = OFF_CTL;
constexpr unsigned CTL_H = OFF_CTL + 8192;
constexpr unsigned CTL_E = OFF_CTL + 16384;
constexpr unsigned CTL_POL = OFF_CTL + 32768;
constexpr unsigned CTL_BYTES = 32768;
constexpr unsigned OFF_DBG = OFF_CTL + 65536;
constexpr unsigned OFF_SXM = 5 * MB;
constexpr unsigned OFF_MP = 7 * MB;
constexpr unsigned FILL2_BYTES = 9 * MB;
constexpr unsigned OFF_XF = 14 * MB;
constexpr unsigned OFF_WD = 16 * MB;
constexpr unsigned OFF_EX = 18 * MB;
constexpr unsigned OFF_LX = 20 * MB;
constexpr unsigned OFF_HF = 21 * MB;
constexpr unsigned OFF_WF = 28 * MB;
constexpr unsigned OFF_KX = 44 * MB;
constexpr unsigned OFF_DX = 72 * MB;
constexpr unsigned WS_USED = 100 * MB;
constexpr unsigned SENT = 0xFFFFFFFFu;
constexpr unsigned SPIN_CAP = 1u << 19;
constexpr int LDS_BYTES = 114688;

DEV unsigned pack2(float lo, float hi) { f32x2 f = {lo, hi}; return __builtin_bit_cast(unsigned, __builtin_convertvector(f, bf16x2)); }
DEV v4u pack8(f32x4 a, f32x4 b) { return (v4u){pack2(a[0], a[1]), pack2(a[2], a[3]), pack2(b[0], b[1]), pack2(b[2], b[3])}; }
DEV unsigned fixs(unsigned x) { return x < 0xFFFFFFFEu ? x : 0xFFFFFFFEu; }
DEV v4u fixs4(v4u v) { return (v4u){fixs(v.x), fixs(v.y), fixs(v.z), fixs(v.w)}; }
DEV float bflo(unsigned u) { return __builtin_bit_cast(float, u << 16); }
DEV float bfhi(unsigned u) { return __builtin_bit_cast(float, u & 0xFFFF0000u); }
DEV f32x4 mma(v4u a, v4u b, f32x4 c) { return __builtin_amdgcn_mfma_f32_16x16x32_bf16(__builtin_bit_cast(bf16x8, a), __builtin_bit_cast(bf16x8, b), c, 0, 0, 0); }
DEV v4u ld16c(rsrc_t r, unsigned so, unsigned vo) { return __builtin_amdgcn_raw_buffer_load_b128(r, vo, so, 16); }
DEV unsigned ld4c(rsrc_t r, unsigned so, unsigned vo) { return __builtin_amdgcn_raw_buffer_load_b32(r, vo, so, 16); }
DEV v4u ld16p(rsrc_t r, unsigned so, unsigned vo) { return __builtin_amdgcn_raw_buffer_load_b128(r, vo, so, 0); }
DEV void st16c(rsrc_t r, unsigned so, unsigned vo, v4u v) { __builtin_amdgcn_raw_buffer_store_b128(v, r, vo + so, 0, 16); }
DEV void st8c(rsrc_t r, unsigned so, unsigned vo, v2u v) { __builtin_amdgcn_raw_buffer_store_b64(v, r, vo, so, 16); }
DEV void st4c(rsrc_t r, unsigned so, unsigned vo, unsigned v) { __builtin_amdgcn_raw_buffer_store_b32(v, r, vo, so, 16); }
DEV void st16p(rsrc_t r, unsigned so, unsigned vo, v4u v) { __builtin_amdgcn_raw_buffer_store_b128(v, r, vo + so, 0, 0); }
DEV unsigned umax(unsigned a, unsigned b) { return a > b ? a : b; }
DEV unsigned max4u(v4u v) { return umax(umax(v.x, v.y), umax(v.z, v.w)); }
DEV unsigned ror8(unsigned x) { return __builtin_amdgcn_update_dpp(0u, x, 0x128, 0xf, 0xf, false); }
DEV float ror8f(float x) { return __builtin_bit_cast(float, ror8(__builtin_bit_cast(unsigned, x))); }
DEV v4u ror8v(v4u v) { return (v4u){ror8(v.x), ror8(v.y), ror8(v.z), ror8(v.w)}; }
DEV float x16max(float x) { unsigned u = __builtin_bit_cast(unsigned, x); auto r = __builtin_amdgcn_permlane16_swap(u, u, false, false); return fmaxf(__builtin_bit_cast(float, (unsigned)r[0]), __builtin_bit_cast(float, (unsigned)r[1])); }
DEV float x32max(float x) { unsigned u = __builtin_bit_cast(unsigned, x); auto r = __builtin_amdgcn_permlane32_swap(u, u, false, false); return fmaxf(__builtin_bit_cast(float, (unsigned)r[0]), __builtin_bit_cast(float, (unsigned)r[1])); }
DEV float x16add(float x) { unsigned u = __builtin_bit_cast(unsigned, x); auto r = __builtin_amdgcn_permlane16_swap(u, u, false, false); return __builtin_bit_cast(float, (unsigned)r[0]) + __builtin_bit_cast(float, (unsigned)r[1]); }
DEV float x32add(float x) { unsigned u = __builtin_bit_cast(unsigned, x); auto r = __builtin_amdgcn_permlane32_swap(u, u, false, false); return __builtin_bit_cast(float, (unsigned)r[0]) + __builtin_bit_cast(float, (unsigned)r[1]); }
DEV float ex2(float x) { return __builtin_amdgcn_exp2f(x); }
DEV float lg2(float x) { return __builtin_amdgcn_logf(x); }
DEV float rcp(float x) { return __builtin_amdgcn_rcpf(x); }
#define CFENCE() asm volatile("" ::: "memory")
#define WG_BAR() do { asm volatile("s_waitcnt lgkmcnt(0)" ::: "memory"); __builtin_amdgcn_s_barrier(); asm volatile("" ::: "memory"); } while (0)
#define WAIT_VM(n) asm volatile("s_waitcnt vmcnt(" #n ")" ::: "memory")
DEV v4u lds16(LAS char* l, unsigned off) { return *(LAS v4u*)(l + off); }
DEV v2u lds8(LAS char* l, unsigned off) { return *(LAS v2u*)(l + off); }
DEV f32x4 lds16f(LAS char* l, unsigned off) { return *(LAS f32x4*)(l + off); }
DEV f32x2 lds8f(LAS char* l, unsigned off) { return *(LAS f32x2*)(l + off); }
DEV v4u onehot(int g, int want) {
  v4u r; const int i = want - 8 * g;
#pragma unroll
  for (int q = 0; q < 4; ++q) r[q] = (i == 2 * q) ? 0x3F80u : ((i == 2 * q + 1) ? 0x3F800000u : 0u);
  return r;
}
DEV unsigned ho_blk(int s, int type, unsigned pol0, unsigned pol1) { return OFF_HO + (unsigned)(s >> 5) * (2 * MB) + (unsigned)((s & 31) * 3 + type) * 16384u + ((s >> 5) ? pol1 : pol0); }

#define STAMP_AT(slot) do {} while (0)

DEV v4u poll16(rsrc_t R, unsigned so, unsigned vo, unsigned& cap) {
  v4u v;
  for (unsigned n = 0;; ++n) {
    v = ld16c(R, so, vo);
    if (__all(max4u(v) != SENT)) break;
    if (n >= cap) { cap = 0; break; }
    CFENCE();
  }
  return v;
}
DEV void wait_cnt(rsrc_t R, unsigned base, int lane, int nlanes, unsigned& cnt, int idx, unsigned target, unsigned& cap, bool nap) {
  for (unsigned n = 0;; ++n) {
    if ((unsigned)__builtin_amdgcn_readlane(cnt, idx) >= target) break;
    if (n >= cap) { cap = 0; break; }
    if (nap) __builtin_amdgcn_s_sleep(8);
    CFENCE();
    cnt = ld4c(R, base, (unsigned)(lane & (nlanes - 1)) * 256u);
  }
  CFENCE();
}
DEV void signal(char* wsb, unsigned off) { __hip_atomic_fetch_add((GAS unsigned*)(wsb + off), 1u, __ATOMIC_RELAXED, __HIP_MEMORY_SCOPE_AGENT); }

struct ProArgs { const unsigned short *x, *mem0, *wq, *wk, *wv, *wo, *wg, *wu, *wd; char* ws; };
DEV char* ws_base(char* ws) { return (char*)(((size_t)ws + 2 * MB - 1) & ~(size_t)(2 * MB - 1)); }
DEV v4u gl16(const unsigned short* p) { return *(const GAS v4u*)p; }

constexpr int PRO_WG_W = 224, PRO_WG_T = 42, PRO_WG_L = 32, PRO_GRID = PRO_WG_W + PRO_WG_T + PRO_WG_L + 1;

__global__ void __launch_bounds__(512, 4) pro_kernel(ProArgs a) {
  const int b = blockIdx.x, w = __builtin_amdgcn_readfirstlane(threadIdx.x >> 6), lane = threadIdx.x & 63, g = lane >> 4, rho = lane & 15;
  char* const wsb = ws_base(a.ws);
  const rsrc_t R = __builtin_amdgcn_make_buffer_rsrc(wsb, 0, WS_USED, 0x00020000);
  const int prow = 8 * (rho >> 2) + (rho & 3);
  if (b < PRO_WG_W) {
    if (w == 7) {
      const v4u sv = {SENT, SENT, SENT, SENT};
      for (unsigned c = b; c < 12288u; c += PRO_WG_W) {
        unsigned off;
        if (c < 1536u) { if (c < 16u) continue; off = OFF_HO + c * 1024u; }
        else if (c < 3072u) off = OFF_HO + 2 * MB + (c - 1536u) * 1024u;
        else off = OFF_SXM + (c - 3072u) * 1024u;
        st16p(R, off, lane * 16, sv);
      }
      return;
    }
    const int isv = b >= 112, bb = isv ? b - 112 : b, h = bb / 7, cb = bb % 7;
    const unsigned short* wa = (isv ? a.wv : a.wk) + (size_t)(64 * w + prow) * 4096 + h * 256 + 8 * g;
    f32x4 acc[4][4];
#pragma unroll
    for (int i = 0; i < 4; ++i)
#pragma unroll
      for (int j = 0; j < 4; ++j) acc[i][j] = (f32x4){0.f, 0.f, 0.f, 0.f};
    const v4u sel0 = onehot(g, rho), sel1 = onehot(g, 16 + rho);
#pragma unroll 1
    for (int es = 0; es < 8; ++es) {
      v4u A[4], B[4];
#pragma unroll
      for (int i = 0; i < 4; ++i) A[i] = gl16(wa + (size_t)(32 * (i >> 1) + 4 * (i & 1)) * 4096 + 32 * es);
      if (!isv) {
#pragma unroll
        for (int j = 0; j < 4; ++j) B[j] = gl16(a.wq + (size_t)(64 * cb + 32 * (j >> 1) + 4 * (j & 1) + prow) * 4096 + h * 256 + 32 * es + 8 * g);
      } else {
#pragma unroll
        for (int cc = 0; cc < 2; ++cc) {
          const unsigned short* p = a.wo + (size_t)(h * 256 + 32 * es + prow) * DM + 64 * cb + 32 * cc + 8 * g;
          const v4u n0 = gl16(p), n1 = gl16(p + 4 * DM);
          const f32x4 z = {0.f, 0.f, 0.f, 0.f};
          B[2 * cc] = pack8(mma(n0, sel0, z), mma(n1, sel0, z));
          B[2 * cc + 1] = pack8(mma(n0, sel1, z), mma(n1, sel1, z));
        }
      }
#pragma unroll
      for (int i = 0; i < 4; ++i)
#pragma unroll
        for (int j = 0; j < 4; ++j) acc[i][j] = mma(A[i], B[j], acc[i][j]);
    }
    const float sc = isv ? 1.f : 0.0625f * 1.4426950408889634f;
#pragma unroll
    for (int j = 0; j < 4; ++j)
#pragma unroll
      for (int u = 0; u < 2; ++u)
        st16p(R, OFF_WF + (unsigned)((isv * 448 + h * 28 + 4 * cb + j) * 14 + 2 * w + u) * 1024u, lane * 16, pack8(acc[2 * u][j] * sc, acc[2 * u + 1][j] * sc));
  } else if (b < PRO_WG_W + PRO_WG_T) {
    const int wv = (b - PRO_WG_W) * 8 + w, mat = wv / 112, r0 = (wv % 112) * 7;
    const v4u sel0 = onehot(g, rho), sel1 = onehot(g, 16 + rho);
    v4u n0[7], n1[7];
#pragma unroll
    for (int i = 0; i < 7; ++i) {
      const int blk = r0 + i;
      const unsigned short* p;
      int ld;
      if (mat < 2) { const int kb = blk / 56, fb = blk % 56; ld = FF; p = (mat ? a.wu : a.wg) + (size_t)(32 * kb + prow) * FF + 32 * fb + 8 * g; }
      else { const int kb = blk / 14, cb = blk % 14; ld = DM; p = a.wd + (size_t)(32 * kb + prow) * DM + 32 * cb + 8 * g; }
      n0[i] = gl16(p); n1[i] = gl16(p + 4 * ld);
    }
#pragma unroll
    for (int i = 0; i < 7; ++i) {
      const int blk = r0 + i;
      const f32x4 z = {0.f, 0.f, 0.f, 0.f};
      const v4u f0 = pack8(mma(n0[i], sel0, z), mma(n1[i], sel0, z)), f1 = pack8(mma(n0[i], sel1, z), mma(n1[i], sel1, z));
      unsigned o0, o1;
      if (mat < 2) { const int kb = blk / 56, fb = blk % 56; o0 = OFF_WF + (unsigned)((896 + 4 * fb + mat) * 14 + kb) * 1024u; o1 = o0 + 2 * 14 * 1024u; }
      else { const int kb = blk / 14, cb = blk % 14; o0 = OFF_WD + (unsigned)(2 * cb * 56 + kb) * 1024u; o1 = o0 + 56 * 1024u; }
      st16p(R, o0, lane * 16, f0); st16p(R, o1, lane * 16, f1);
    }
  } else if (b < PRO_WG_W + PRO_WG_T + PRO_WG_L) {
    const int row0 = ((b - PRO_WG_W - PRO_WG_T) * 8 + w) * 8;
    const bool act = lane < 56;
    v4u v[8];
#pragma unroll
    for (int i = 0; i < 8; ++i) v[i] = act ? gl16(a.x + (size_t)(row0 + i) * DM + lane * 8) : (v4u){0u, 0u, 0u, 0u};
#pragma unroll
    for (int i = 0; i < 8; ++i) {
      float f[8];
#pragma unroll
      for (int q = 0; q < 4; ++q) { f[2 * q] = bflo(v[i][q]); f[2 * q + 1] = bfhi(v[i][q]); }
      float s = 0.f;
#pragma unroll
      for (int q = 0; q < 8; ++q) s += f[q];
#pragma unroll
      for (int o = 1; o < 64; o <<= 1) s += __shfl_xor(s, o);
      const float mean = s * (1.f / DM);
      float vs = 0.f;
#pragma unroll
      for (int q = 0; q < 8; ++q) { f[q] = act ? f[q] - mean : 0.f; vs += f[q] * f[q]; }
#pragma unroll
      for (int o = 1; o < 64; o <<= 1) vs += __shfl_xor(vs, o);
      const float rs = 1.f / sqrtf(vs * (1.f / DM) + 1e-5f);
      const int row = row0 + i;
      const v4u o4 = {pack2(f[0] * rs, f[1] * rs), pack2(f[2] * rs, f[3] * rs), pack2(f[4] * rs, f[5] * rs), pack2(f[6] * rs, f[7] * rs)};
      if (act) st16p(R, OFF_XF + (unsigned)((row >> 4) * 14) * 1024u + (unsigned)(row & 15) * 16u, (unsigned)(lane >> 2) * 1024u + (unsigned)(lane & 3) * 256u, o4);
    }
  } else {
    if (w < 2) {
      unsigned best[2] = {~0u, ~0u};
      for (int i = 0; i < 16; ++i) {
        const char* p = wsb + OFF_HO + (size_t)w * (2 * MB) + (size_t)(100 + (i >> 1)) * 16384 + (i & 1) * 8192 + 1024;
        unsigned v;
        const unsigned long long c0 = __builtin_amdgcn_s_memtime();
        asm volatile("s_waitcnt lgkmcnt(0)\n\tglobal_load_dword %0, %1, off sc1\n\ts_waitcnt vmcnt(0)" : "=&v"(v) : "v"(p) : "memory");
        const unsigned long long c1 = __builtin_amdgcn_s_memtime();
        const unsigned d = (unsigned)(c1 - c0);
        if (i & 1) best[1] = d < best[1] ? d : best[1]; else best[0] = d < best[0] ? d : best[0];
      }
      if (lane == 0) __builtin_amdgcn_raw_buffer_store_b32(best[1] < best[0] ? 8192u : 0u, R, CTL_POL + w * 4, 0, 0);
    } else if (w == 2) {
      for (unsigned c = 0; c < CTL_BYTES / 1024; ++c) st16p(R, OFF_CTL + c * 1024u, lane * 16, (v4u){0u, 0u, 0u, 0u});
    } else if (w == 3) {
      for (int c = 0; c < 8; ++c) {
        v4u v = {SENT, SENT, SENT, SENT};
        if (c < 7) { const int pc = c * 64 + lane; v = fixs4(gl16(a.mem0 + (size_t)(pc & 7) * DM + (pc >> 3) * 8)); }
        st16p(R, OFF_HO + c * 1024u, lane * 16, v);
        st16p(R, OFF_HO + 8192u + c * 1024u, lane * 16, v);
      }
    }
  }
}

struct MegaArgs { const unsigned short* x; unsigned short* out; char* ws; };

DEV void role_v(rsrc_t R, LAS char* L, int v, int w, int lane, unsigned pol0, unsigned pol1) {
  const int g = lane >> 4, rho = lane & 15, hd = rho >> 3, r = rho & 7;
  constexpr unsigned L_M = 0, L_Z = 7168, L_RED = 14336;
  unsigned cap = SPIN_CAP;
  v4u C[2][14];
#pragma unroll
  for (int a = 0; a < 2; ++a)
#pragma unroll
    for (int k = 0; k < 14; ++k) C[a][k] = ld16p(R, OFF_WF + (unsigned)((448 + (2 * w + a) * 28 + v) * 14 + k) * 1024u, lane * 16);
  if (w < 7) *(LAS v4u*)(L + L_Z + w * 1024 + lane * 16) = (v4u){0u, 0u, 0u, 0u};
  const v4u ksel = onehot(0, hd ? -1 : r);
  v4u msk;
  if (g < 2) msk = hd ? (v4u){0u, 0u, ~0u, ~0u} : (v4u){~0u, ~0u, 0u, 0u};
  else { const v4u o = onehot(0, ((r >> 2) == g - 2) ? 4 * hd + (r & 3) : -1);
#pragma unroll
    for (int q = 0; q < 4; ++q) msk[q] = (o[q] & 0xFFFFu) ? 0xFFFFu : (o[q] ? 0xFFFF0000u : 0u); }
  const float wgt = g < 2 ? 1.f : 0.125f;
  const unsigned mrd = (hd ? L_Z : L_M) + (unsigned)(g * 128 + r * 16);
  unsigned cnt = 0;
  v4u Dn[2];
  wait_cnt(R, CTL_KV, lane, 32, cnt, 0, 112, cap, false);
#pragma unroll
  for (int a = 0; a < 2; ++a) Dn[a] = ld16c(R, OFF_DX + (unsigned)((2 * w + a) * 28 + v) * 1024u, lane * 16);
  for (int s = 0; s < NSEG; ++s) {
    const unsigned bm = ho_blk(s, 0, pol0, pol1);
    if (w < 7) { const v4u mv = poll16(R, bm + w * 1024, lane * 16, cap); *(LAS v4u*)(L + L_M + w * 1024 + lane * 16) = mv; }
    WG_BAR();
    if (v == 0 && w == 0) STAMP_AT(s * 16 + 0);
    const v4u D0 = Dn[0], D1 = Dn[1];
    if (s + 2 < NSEG) {
      wait_cnt(R, CTL_KV, lane, 32, cnt, (s + 1) >> 1, 112, cap, false);
#pragma unroll
      for (int a = 0; a < 2; ++a) Dn[a] = ld16c(R, OFF_DX + (unsigned)(((s + 1) * 16 + 2 * w + a) * 28 + v) * 1024u, lane * 16);
      cnt = ld4c(R, CTL_KV, (unsigned)(lane & 31) * 256u);
    }
    f32x4 acc0 = {0.f, 0.f, 0.f, 0.f}, acc1 = acc0;
#pragma unroll
    for (int k = 0; k < 14; ++k) { const v4u mf = lds16(L, mrd + k * 512); acc0 = mma(mf, C[0][k], acc0); acc1 = mma(mf, C[1][k], acc1); }
    if (g < 2) st16c(R, OFF_MP + (unsigned)((s * 8 + w) * 28 + v) * 512u, lane * 16, fixs4(pack8(acc0, acc1)));
    if (s == NSEG - 1) break;
    if (v == 0 && w == 0) STAMP_AT(s * 16 + 1);
    v4u ef; unsigned lse;
    { const unsigned eo = ho_blk(s, 2, pol0, pol1) + (unsigned)w * 1024u, lo = bm + 7168u + (unsigned)w * 128u;
      for (unsigned n = 0;; ++n) {
        ef = ld16c(R, eo, (1 - hd) * 512 + (g * 8 + r) * 16); lse = ld4c(R, lo, hd * 64 + r * 4);
        if (__all(umax(max4u(ef), lse) != SENT)) break;
        if (n >= cap) { cap = 0; break; }
        CFENCE();
      } }
    if (v == 0 && w == 0) STAMP_AT(s * 16 + 2);
    v4u ea, eb;
#pragma unroll
    for (int q = 0; q < 4; ++q) { ea[q] = hd ? ef[q] : 0u; eb[q] = __builtin_amdgcn_update_dpp(0u, ef[q], 0x128, 0xf, 0xc, false); }
    acc0 = mma(ea, D0, acc0); acc1 = mma(eb, D1, acc1);
    const v4u za = pack8(acc0, acc1);
    const float lf = g < 2 ? 0.f : __builtin_bit_cast(float, lse);
    const v4u sp = poll16(R, ho_blk(s, 1, pol0, pol1) + w * 1024, lane * 16, cap);
    if (v == 0 && w == 0) STAMP_AT(s * 16 + 3);
    const f32x4 val = mma(ksel, sp, (f32x4){lf, lf, lf, lf});
    const float mx = x32max(x16max(fmaxf(fmaxf(val[0], val[1]), fmaxf(val[2], val[3]))));
    const float e0 = ex2(val[0] - mx), e1 = ex2(val[1] - mx), e2 = ex2(val[2] - mx), e3 = ex2(val[3] - mx);
    const unsigned p01 = pack2(e0, e1), p23 = pack2(e2, e3);
    f32x4 o = mma(za, (v4u){p01 & msk.x, p23 & msk.y, p01 & msk.z, p23 & msk.w}, (f32x4){0.f, 0.f, 0.f, 0.f});
    const float inv = rcp(x32add(x16add(((e0 + e1) + (e2 + e3)) * wgt)));
#pragma unroll
    for (int i = 0; i < 4; ++i) { o[i] *= inv; o[i] += ror8f(o[i]); }
    if (!hd) *(LAS f32x4*)(L + L_RED + w * 512 + (g >> 1) * 256 + r * 32 + (g & 1) * 16) = o;
    WG_BAR();
    if (w == 7) {
      f32x2 t[8];
#pragma unroll
      for (int i = 0; i < 8; ++i) t[i] = lds8f(L, L_RED + i * 512 + lane * 8);
      const f32x2 u = ((t[0] + t[1]) + (t[2] + t[3])) + ((t[4] + t[5]) + (t[6] + t[7]));
      st4c(R, ho_blk(s + 1, 0, pol0, pol1) + v * 256, lane * 4, fixs(pack2(u[0], u[1])));
      if (v == 0) STAMP_AT(s * 16 + 4);
    }
  }
}

DEV void role_q(rsrc_t R, LAS char* L, int h, int p, int w, int lane, unsigned pol0, unsigned pol1) {
  const int g = lane >> 4, rho = lane & 15, hd = rho >> 3, r = rho & 7;
  constexpr unsigned L_M = 0, L_RED = 7168, L_RED2 = 9216, L_X = 16384;
  unsigned cap = SPIN_CAP;
  if (w < 7) {
    v4u A[7][2];
#pragma unroll
    for (int t = 0; t < 7; ++t)
#pragma unroll
      for (int u = 0; u < 2; ++u) A[t][u] = ld16p(R, OFF_WF + (unsigned)((h * 28 + (t < 6 ? 6 * p + t : 24 + p)) * 14 + 2 * w + u) * 1024u, lane * 16);
    const unsigned mo = (unsigned)(((2 * w + hd) * 4 + g) * 128 + r * 16);
    const unsigned mb = L_M + (unsigned)((3 * p * 4 + g) * 128 + r * 16), mh = L_M + (unsigned)(((12 + (p >> 1)) * 4 + g) * 128 + r * 16);
    for (int s = 0; s < NSEG; ++s) {
      const v4u f0 = poll16(R, ho_blk(s, 0, pol0, pol1), mo, cap);
      if (h == 0 && p == 0 && w == 0) STAMP_AT(s * 16 + 5);
      *(LAS v4u*)(L + L_M + mo) = f0;
      const v4u f1 = ror8v(f0);
      f32x4 acc[7];
#pragma unroll
      for (int t = 0; t < 7; ++t) acc[t] = mma(A[t][0], f0, (f32x4){0.f, 0.f, 0.f, 0.f});
      WG_BAR();
      const v4u b0 = lds16(L, mb), b1 = lds16(L, mb + 512), b2 = lds16(L, mb + 1024), bh = lds16(L, mh);
#pragma unroll
      for (int t = 0; t < 7; ++t) acc[t] = mma(A[t][1], f1, acc[t]);
      const v4u k0 = pack8(acc[0], acc[1]), k1 = pack8(acc[2], acc[3]), k2 = pack8(acc[4], acc[5]);
      const unsigned ka = pack2(acc[6][0], acc[6][1]), kb = pack2(acc[6][2], acc[6][3]);
      const v4u kh = (p & 1) ? (v4u){0u, 0u, ka, kb} : (v4u){ka, kb, 0u, 0u};
      f32x4 sm = mma(k0, b0, (f32x4){0.f, 0.f, 0.f, 0.f});
      sm = mma(k1, b1, sm); sm = mma(k2, b2, sm); sm = mma(kh, bh, sm);
      if (g < 2 && !hd) *(LAS f32x4*)(L + L_RED + w * 256 + r * 32 + g * 16) = sm;
      WG_BAR();
      const unsigned xb = L_X + (unsigned)(s & 1) * 8192u;
#pragma unroll
      for (int tt = 0; tt < 2; ++tt) {
        f32x4 sx = mma(k0, lds16(L, xb + tt * 4096 + lane * 16), (f32x4){0.f, 0.f, 0.f, 0.f});
        sx = mma(k1, lds16(L, xb + tt * 4096 + 1024 + lane * 16), sx);
        sx = mma(k2, lds16(L, xb + tt * 4096 + 2048 + lane * 16), sx);
        sx = mma(kh, lds16(L, xb + tt * 4096 + 3072 + lane * 16), sx);
        if (g < 2) *(LAS f32x4*)(L + L_RED2 + w * 1024 + (tt * 32 + rho * 2 + g) * 16) = sx;
      }
      WG_BAR();
    }
  } else {
    v4u xf[8];
#pragma unroll
    for (int i = 0; i < 8; ++i) xf[i] = ld16p(R, OFF_XF + (unsigned)((i >> 2) * 14 + ((i & 3) < 3 ? 3 * p + (i & 3) : 12 + (p >> 1))) * 1024u, lane * 16);
#pragma unroll
    for (int i = 0; i < 8; ++i) *(LAS v4u*)(L + L_X + i * 1024 + lane * 16) = xf[i];
    for (int s = 0; s < NSEG; ++s) {
      WG_BAR();
      WG_BAR();
      { f32x2 t[7];
#pragma unroll
        for (int i = 0; i < 7; ++i) t[i] = lds8f(L, L_RED + i * 256 + (lane & 31) * 8);
        const f32x2 u = ((t[0] + t[1]) + (t[2] + t[3])) + ((t[4] + t[5]) + t[6]);
        if (lane < 32) st4c(R, ho_blk(s, 1, pol0, pol1) + (unsigned)((h >> 1) * 1024 + p * 256 + (h & 1) * 128), lane * 4, fixs(pack2(u[0], u[1])));
        if (h == 0 && p == 0) STAMP_AT(s * 16 + 6); }
      if (s + 1 < NSEG) {
#pragma unroll
        for (int i = 0; i < 8; ++i) xf[i] = ld16p(R, OFF_XF + (unsigned)(((s + 1) * 2 + (i >> 2)) * 14 + ((i & 3) < 3 ? 3 * p + (i & 3) : 12 + (p >> 1))) * 1024u, lane * 16);
      }
      WG_BAR();
      { f32x4 t[7];
#pragma unroll
        for (int i = 0; i < 7; ++i) t[i] = lds16f(L, L_RED2 + i * 1024 + lane * 16);
        const f32x4 u = ((t[0] + t[1]) + (t[2] + t[3])) + ((t[4] + t[5]) + t[6]);
        st8c(R, OFF_SXM + (unsigned)((s * 16 + h) * 8 + p) * 256u, (lane >> 5) * 1024 + (lane & 31) * 8, (v2u){fixs(pack2(u[0], u[1])), fixs(pack2(u[2], u[3]))}); }
      if (s + 1 < NSEG) {
#pragma unroll
        for (int i = 0; i < 8; ++i) *(LAS v4u*)(L + L_X + ((s + 1) & 1) * 8192 + i * 1024 + lane * 16) = xf[i];
      }
    }
  }
}

DEV void role_x(rsrc_t R, LAS char* L, int h, int w, int lane, unsigned pol0, unsigned pol1) {
  const int g = lane >> 4, rho = lane & 15, hd = rho >> 3, r = rho & 7;
  unsigned cap = SPIN_CAP;
  if (w < 7) {
    const unsigned mo = (unsigned)(((2 * w + hd) * 4 + g) * 128 + r * 16);
    unsigned cnt = 0;
    v4u kx[2][2];
    wait_cnt(R, CTL_KV, lane, 32, cnt, 0, 112, cap, false);
#pragma unroll
    for (int i = 0; i < 4; ++i) kx[i >> 1][i & 1] = ld16c(R, OFF_KX + (unsigned)((h * 2 + (i >> 1)) * 14 + 2 * w + (i & 1)) * 1024u, lane * 16);
    for (int s = 0; s < NSEG - 1; ++s) {
      const v4u f0 = poll16(R, ho_blk(s, 0, pol0, pol1), mo, cap);
      if (h == 0 && w == 0) STAMP_AT(s * 16 + 7);
      const v4u f1 = ror8v(f0);
      f32x4 a0 = mma(kx[0][0], f0, (f32x4){0.f, 0.f, 0.f, 0.f}), a1 = mma(kx[1][1], f0, (f32x4){0.f, 0.f, 0.f, 0.f});
      a0 = mma(kx[0][1], f1, a0); a1 = mma(kx[1][0], f1, a1);
      f32x4 o;
#pragma unroll
      for (int i = 0; i < 4; ++i) o[i] = hd ? a1[i] : a0[i];
      *(LAS f32x4*)(L + w * 1024 + lane * 16) = o;
      WG_BAR();
      if (s + 2 < NSEG) {
        wait_cnt(R, CTL_KV, lane, 32, cnt, (s + 1) >> 1, 112, cap, false);
#pragma unroll
        for (int i = 0; i < 4; ++i) kx[i >> 1][i & 1] = ld16c(R, OFF_KX + (unsigned)((((s + 1) * 16 + h) * 2 + (i >> 1)) * 14 + 2 * w + (i & 1)) * 1024u, lane * 16);
        cnt = ld4c(R, CTL_KV, (unsigned)(lane & 31) * 256u);
      }
    }
  } else {
    for (int s = 0; s < NSEG - 1; ++s) {
      WG_BAR();
      f32x4 t[7];
#pragma unroll
      for (int i = 0; i < 7; ++i) t[i] = lds16f(L, i * 1024 + lane * 16);
      const f32x4 u = ((t[0] + t[1]) + (t[2] + t[3])) + ((t[4] + t[5]) + t[6]);
      float mx = fmaxf(fmaxf(u[0], u[1]), fmaxf(u[2], u[3]));
      mx = fmaxf(mx, ror8f(mx)); mx = x32max(x16max(mx));
      const float e0 = ex2(u[0] - mx), e1 = ex2(u[1] - mx), e2 = ex2(u[2] - mx), e3 = ex2(u[3] - mx);
      float l = (e0 + e1) + (e2 + e3);
      l += ror8f(l); l = x32add(x16add(l));
      const float inv = rcp(l);
      st8c(R, ho_blk(s, 2, pol0, pol1) + (unsigned)h * 512u, (g * 8 + r) * 16 + hd * 8, (v2u){fixs(pack2(e0 * inv, e1 * inv)), fixs(pack2(e2 * inv, e3 * inv))});
      if (lane < 8) st4c(R, ho_blk(s, 0, pol0, pol1) + 7168u + (unsigned)h * 64u, lane * 4, fixs(__builtin_bit_cast(unsigned, mx + lg2(l))));
      if (h == 0) STAMP_AT(s * 16 + 8);
    }
  }
}

DEV void dma1k(const char* wsb, unsigned off, LAS char* dst, int lane) {
  __builtin_amdgcn_global_load_lds((const GAS unsigned*)(wsb + off + lane * 16), (LAS unsigned*)dst, 16, 0, 0);
}

template <int TYPE>
DEV void role_g(rsrc_t R, char* wsb, LAS char* L, int gi, int w, int lane) {
  const int wc = w & 3, ws = w >> 2, g = lane >> 4, rho = lane & 15, t0 = 8 * gi + 2 * wc;
#pragma unroll
  for (int i = 0; i < 7; ++i) dma1k(wsb, OFF_XF + (unsigned)(w * 7 + i) * 1024u, L + (w * 7 + i) * 1024, lane);
  v4u W[2][14];
#pragma unroll
  for (int c = 0; c < 2; ++c)
#pragma unroll
    for (int k = 0; k < 14; ++k) W[c][k] = ld16p(R, OFF_WF + (unsigned)((t0 + c) * 14 + k) * 1024u, lane * 16);
  WAIT_VM(0);
  WG_BAR();
  const unsigned ctl = TYPE == 2 ? CTL_H : CTL_KV;
  for (int it = 0; it < 32; ++it) {
    LAS char* cur = L + (it & 1) * 57344 + ws * 28672 + lane * 16;
    if (it + 1 < 32) {
#pragma unroll
      for (int i = 0; i < 7; ++i) dma1k(wsb, OFF_XF + (unsigned)((it + 1) * 56 + w * 7 + i) * 1024u, L + ((it + 1) & 1) * 57344 + (w * 7 + i) * 1024, lane);
    }
    f32x4 acc[2][2];
#pragma unroll
    for (int c = 0; c < 2; ++c) acc[c][0] = acc[c][1] = (f32x4){0.f, 0.f, 0.f, 0.f};
#pragma unroll
    for (int k = 0; k < 14; ++k) {
      const v4u x0 = *(LAS v4u*)(cur + k * 1024), x1 = *(LAS v4u*)(cur + 14336 + k * 1024);
#pragma unroll
      for (int c = 0; c < 2; ++c) {
        if (TYPE == 1) { acc[c][0] = mma(x0, W[c][k], acc[c][0]); acc[c][1] = mma(x1, W[c][k], acc[c][1]); }
        else { acc[c][0] = mma(W[c][k], x0, acc[c][0]); acc[c][1] = mma(W[c][k], x1, acc[c][1]); }
      }
    }
    const int s = 2 * it + ws;
    if (TYPE == 0) {
      const int h = t0 / 28, u = (t0 % 28) >> 1;
#pragma unroll
      for (int tt = 0; tt < 2; ++tt) st16c(R, OFF_KX + (unsigned)(((s * 16 + h) * 2 + tt) * 14 + u) * 1024u, lane * 16, pack8(acc[0][tt], acc[1][tt]));
    } else if (TYPE == 1) {
#pragma unroll
      for (int c = 0; c < 2; ++c) st16c(R, OFF_DX + (unsigned)(s * 448 + (t0 - 448) + c) * 1024u, lane * 16, pack8(acc[c][0], acc[c][1]));
    } else {
      const int F = (t0 - 896) >> 1;
      const f32x4 ha = acc[0][0] * acc[1][0], hb = acc[0][1] * acc[1][1];
      const auto r0 = __builtin_amdgcn_permlane16_swap(pack2(ha[0], ha[1]), pack2(hb[0], hb[1]), false, false);
      const auto r1 = __builtin_amdgcn_permlane16_swap(pack2(ha[2], ha[3]), pack2(hb[2], hb[3]), false, false);
      st16c(R, OFF_HF + (unsigned)(s * 112 + (F >> 1)) * 1024u + (unsigned)(F & 1) * 512u, (g & 1) * 57344 + ((g >> 1) * 16 + rho) * 16, (v4u){r0[0], r1[0], r0[1], r1[1]});
    }
    if (it == 0 || it == 31) WAIT_VM(0); else if (TYPE == 2) WAIT_VM(1); else WAIT_VM(2);
    WG_BAR();
    if (w == 0 && lane == 0) {
      if (it == 0) signal(wsb, ctl);
      else if (it > 1) signal(wsb, ctl + (it - 1) * 256);
      if (it == 31) signal(wsb, ctl + 31 * 256);
    }
  }
}

DEV void role_f1(rsrc_t R, char* wsb, LAS char* L, int task, int w, int lane, unsigned& cap) {
  const int s = task >> 1, h = (task & 1) * 8 + w, g = lane >> 4, rho = lane & 15;
  if (w < 7) {
#pragma unroll
    for (int i = 0; i < 4; ++i) dma1k(wsb, OFF_XF + (unsigned)(s * 28 + w * 4 + i) * 1024u, L + (w * 4 + i) * 1024, lane);
  }
  unsigned cnt = 0;
  wait_cnt(R, CTL_KV, lane, 32, cnt, s >> 1, 112, cap, true);
  v4u kx[28];
#pragma unroll
  for (int i = 0; i < 28; ++i) kx[i] = ld16c(R, OFF_KX + (unsigned)((s * 16 + h) * 28 + i) * 1024u, lane * 16);
  WAIT_VM(0);
  WG_BAR();
  f32x4 a00 = {0.f, 0.f, 0.f, 0.f}, a01 = a00, a11 = a00;
#pragma unroll
  for (int k = 0; k < 14; ++k) {
    const v4u x0 = lds16(L, k * 1024 + lane * 16), x1 = lds16(L, 14336 + k * 1024 + lane * 16);
    a00 = mma(kx[k], x0, a00); a01 = mma(kx[k], x1, a01); a11 = mma(kx[14 + k], x1, a11);
  }
  const float NEG = -3.0e38f;
#pragma unroll
  for (int i = 0; i < 4; ++i) { const bool ok = 4 * g + i <= rho; a00[i] = ok ? a00[i] : NEG; a11[i] = ok ? a11[i] : NEG; }
  const float m0 = x32max(x16max(fmaxf(fmaxf(a00[0], a00[1]), fmaxf(a00[2], a00[3]))));
  const float m1 = x32max(x16max(fmaxf(fmaxf(fmaxf(a01[0], a01[1]), fmaxf(a01[2], a01[3])), fmaxf(fmaxf(a11[0], a11[1]), fmaxf(a11[2], a11[3])))));
#pragma unroll
  for (int i = 0; i < 4; ++i) { a00[i] = ex2(a00[i] - m0); a01[i] = ex2(a01[i] - m1); a11[i] = ex2(a11[i] - m1); }
  const float l0 = x32add(x16add((a00[0] + a00[1]) + (a00[2] + a00[3])));
  const float l1 = x32add(x16add(((a01[0] + a01[1]) + (a01[2] + a01[3])) + ((a11[0] + a11[1]) + (a11[2] + a11[3]))));
  const float i0 = rcp(l0), i1 = rcp(l1);
  st16c(R, OFF_EX + (unsigned)((s * 16 + h) * 2) * 1024u, lane * 16, pack8(a00 * i0, (f32x4){0.f, 0.f, 0.f, 0.f}));
  st16c(R, OFF_EX + (unsigned)((s * 16 + h) * 2 + 1) * 1024u, lane * 16, pack8(a01 * i1, a11 * i1));
  if (g < 2) st4c(R, OFF_LX + (unsigned)(s * 16 + h) * 128u, lane * 4, __builtin_bit_cast(unsigned, g ? m1 + lg2(l1) : m0 + lg2(l0)));
  WAIT_VM(0);
  WG_BAR();
  if (w == 0 && lane == 0) signal(wsb, CTL_E + s * 256);
}

DEV v4u scale8(v4u e, float a) {
  v4u r;
#pragma unroll
  for (int q = 0; q < 4; ++q) r[q] = pack2(bflo(e[q]) * a, bfhi(e[q]) * a);
  return r;
}

DEV void role_f2(rsrc_t R, const MegaArgs& a, LAS char* L, int q, int grp, int w, int lane, unsigned& cap) {
  const int g = lane >> 4, rho = lane & 15;
  v4u Wd[7][4];
#pragma unroll
  for (int k = 0; k < 7; ++k)
#pragma unroll
    for (int c = 0; c < 4; ++c) Wd[k][c] = ld16p(R, OFF_WD + (unsigned)((4 * q + c) * 56 + 7 * w + k) * 1024u, lane * 16);
  const v4u sel0 = onehot(0, rho < 8 ? rho : -1), sel1 = onehot(0, rho >= 8 ? rho - 8 : -1);
  for (int s = grp; s < NSEG; s += 21) {
    unsigned cnt = 0;
    wait_cnt(R, CTL_H, lane, 32, cnt, s >> 1, 28, cap, true);
    f32x4 acc[4][2];
#pragma unroll
    for (int c = 0; c < 4; ++c) acc[c][0] = acc[c][1] = (f32x4){0.f, 0.f, 0.f, 0.f};
    { v4u hf[14];
#pragma unroll
      for (int i = 0; i < 14; ++i) hf[i] = ld16c(R, OFF_HF + (unsigned)((s * 2 + i / 7) * 56 + 7 * w + i % 7) * 1024u, lane * 16);
#pragma unroll
      for (int k = 0; k < 7; ++k)
#pragma unroll
        for (int c = 0; c < 4; ++c) { acc[c][0] = mma(Wd[k][c], hf[k], acc[c][0]); acc[c][1] = mma(Wd[k][c], hf[7 + k], acc[c][1]); } }
    cnt = 0;
    wait_cnt(R, CTL_E, lane, 64, cnt, s, 2, cap, true);
    cnt = 0;
    wait_cnt(R, CTL_KV, lane, 32, cnt, s >> 1, 112, cap, true);
    v4u D[2][4], E[2][2]; float lse[2];
#pragma unroll
    for (int hh = 0; hh < 2; ++hh) {
      const unsigned sh = (unsigned)(s * 16 + 2 * w + hh);
#pragma unroll
      for (int c = 0; c < 4; ++c) D[hh][c] = ld16c(R, OFF_DX + (sh * 28 + 4 * q + c) * 1024u, lane * 16);
      E[hh][0] = ld16c(R, OFF_EX + sh * 2048u, lane * 16); E[hh][1] = ld16c(R, OFF_EX + sh * 2048u + 1024u, lane * 16);
      lse[hh] = __builtin_bit_cast(float, ld4c(R, OFF_LX + sh * 128u, ((g >> 1) * 16 + rho) * 4));
    }
    v4u sx[4], mp[4];
    for (unsigned n = 0;; ++n) {
      unsigned m = 0;
#pragma unroll
      for (int i = 0; i < 4; ++i) {
        sx[i] = ld16c(R, OFF_SXM + (unsigned)((s * 16 + 2 * w) * 2 + i) * 1024u, lane * 16);
        mp[i] = ld16c(R, OFF_MP + (unsigned)((s * 8 + w) * 28 + 4 * q + i) * 512u, (lane & 31) * 16);
      }
#pragma unroll
      for (int i = 0; i < 4; ++i) m = umax(m, umax(max4u(sx[i]), max4u(mp[i])));
      if (__all(m != SENT)) break;
      if (n >= cap) { cap = 0; break; }
      __builtin_amdgcn_s_sleep(16);
      CFENCE();
    }
    v4u bm; float al[2][2];
#pragma unroll
    for (int hh = 0; hh < 2; ++hh) {
      f32x4 sv = mma(sel0, sx[2 * hh], (f32x4){0.f, 0.f, 0.f, 0.f});
      sv = mma(sel1, sx[2 * hh + 1], sv);
      const float mx = fmaxf(x16max(fmaxf(fmaxf(sv[0], sv[1]), fmaxf(sv[2], sv[3]))), lse[hh]);
      const float e0 = ex2(sv[0] - mx), e1 = ex2(sv[1] - mx), e2 = ex2(sv[2] - mx), e3 = ex2(sv[3] - mx), ea = ex2(lse[hh] - mx);
      const float inv = rcp(x16add((e0 + e1) + (e2 + e3)) + ea);
      bm[2 * hh] = pack2(e0 * inv, e1 * inv); bm[2 * hh + 1] = pack2(e2 * inv, e3 * inv);
      const unsigned au = __builtin_bit_cast(unsigned, ea * inv);
      const auto rr = __builtin_amdgcn_permlane32_swap(au, au, false, false);
      al[hh][0] = __builtin_bit_cast(float, (unsigned)rr[0]); al[hh][1] = __builtin_bit_cast(float, (unsigned)rr[1]);
    }
    const v4u zz = {0u, 0u, 0u, 0u};
    const v4u b0 = g < 2 ? bm : zz, b1 = g < 2 ? zz : bm;
#pragma unroll
    for (int c = 0; c < 4; ++c) { acc[c][0] = mma(mp[c], b0, acc[c][0]); acc[c][1] = mma(mp[c], b1, acc[c][1]); }
#pragma unroll
    for (int hh = 0; hh < 2; ++hh)
#pragma unroll
      for (int tt = 0; tt < 2; ++tt) {
        const v4u es = scale8(E[hh][tt], al[hh][tt]);
#pragma unroll
        for (int c = 0; c < 4; ++c) acc[c][tt] = mma(D[hh][c], es, acc[c][tt]);
      }
#pragma unroll
    for (int tt = 0; tt < 2; ++tt)
#pragma unroll
      for (int c = 0; c < 4; ++c) *(LAS f32x4*)(L + w * 8192 + (tt * 4 + c) * 1024 + lane * 16) = acc[c][tt];
    const size_t oi = (size_t)(s * 32 + (w >> 2) * 16 + rho) * DM + 64 * q + 16 * (w & 3) + 4 * g;
    const v2u xv = *(const GAS v2u*)(a.x + oi);
    WG_BAR();
    f32x4 t[8];
#pragma unroll
    for (int i = 0; i < 8; ++i) t[i] = lds16f(L, i * 8192 + w * 1024 + lane * 16);
    const f32x4 u = ((t[0] + t[1]) + (t[2] + t[3])) + ((t[4] + t[5]) + (t[6] + t[7]));
    *(GAS v2u*)(a.out + oi) = (v2u){pack2(u[0] + bflo(xv.x), u[1] + bfhi(xv.x)), pack2(u[2] + bflo(xv.y), u[3] + bfhi(xv.y))};
    WG_BAR();
  }
}

__global__ void __launch_bounds__(512, 2) mega_kernel(MegaArgs a) {
  extern __shared__ __attribute__((aligned(16))) char smem[];
  LAS char* L = (LAS char*)smem;
  const int b = blockIdx.x, w = __builtin_amdgcn_readfirstlane(threadIdx.x >> 6), lane = threadIdx.x & 63;
  char* const wsb = ws_base(a.ws);
  const rsrc_t R = __builtin_amdgcn_make_buffer_rsrc(wsb, 0, WS_USED, 0x00020000);
  const int side = (b >> 2) & 1, idx = (b >> 3) * 4 + (b & 3);
  if (side == 0 && idx < 108) {
    const unsigned pol0 = __builtin_amdgcn_readfirstlane(__builtin_amdgcn_raw_buffer_load_b32(R, CTL_POL, 0, 0)) & 8192u;
    const unsigned pol1 = __builtin_amdgcn_readfirstlane(__builtin_amdgcn_raw_buffer_load_b32(R, CTL_POL + 4, 0, 0)) & 8192u;
    if (idx < 28) role_v(R, L, idx, w, lane, pol0, pol1);
    else if (idx < 44) role_x(R, L, idx - 28, w, lane, pol0, pol1);
    else role_q(R, L, (idx - 44) >> 2, (idx - 44) & 3, w, lane, pol0, pol1);
    return;
  }
  const int bi = side ? idx : 128 + idx - 108;
  if (bi == 0 && w == 0) STAMP_AT(1024 + 0);
  if (bi < 56) role_g<0>(R, wsb, L, bi, w, lane);
  else if (bi < 112) role_g<1>(R, wsb, L, bi, w, lane);
  else if (bi < 140) role_g<2>(R, wsb, L, bi, w, lane);
  if (bi == 0 && w == 0) STAMP_AT(1024 + 1);
  unsigned cap = SPIN_CAP;
  if (bi < 128) role_f1(R, wsb, L, bi, w, lane, cap);
  if (bi == 0 && w == 0) STAMP_AT(1024 + 2);
  if (bi < 147) role_f2(R, a, L, bi % 7, bi / 7, w, lane, cap);
  if (bi == 0 && w == 0) STAMP_AT(1024 + 3);
}

extern "C" void kernel_launch(void* const* d_in, const int* in_sizes, int n_in, void* d_out, int out_size, void* d_ws, size_t ws_size, hipStream_t stream) {
  ProArgs p;
  p.x = (const unsigned short*)d_in[0]; p.mem0 = (const unsigned short*)d_in[1];
  p.wq = (const unsigned short*)d_in[2]; p.wk = (const unsigned short*)d_in[3]; p.wv = (const unsigned short*)d_in[4]; p.wo = (const unsigned short*)d_in[5];
  p.wg = (const unsigned short*)d_in[8]; p.wu = (const unsigned short*)d_in[9]; p.wd = (const unsigned short*)d_in[10];
  p.ws = (char*)d_ws;
  pro_kernel<<<PRO_GRID, 512, 0, stream>>>(p);
  MegaArgs m; m.x = p.x; m.out = (unsigned short*)d_out; m.ws = (char*)d_ws;
  (void)hipFuncSetAttribute((const void*)mega_kernel, hipFuncAttributeMaxDynamicSharedMemorySize, LDS_BYTES);
  mega_kernel<<<256, 512, LDS_BYTES, stream>>>(m);
}
```

```cpp
#include <hip/hip_runtime.h>
#include <hip/hip_bf16.h>

typedef unsigned v4u __attribute__((ext_vector_type(4)));
typedef unsigned v2u __attribute__((ext_vector_type(2)));
typedef float f32x4 __attribute__((ext_vector_type(4)));
typedef float f32x2 __attribute__((ext_vector_type(2)));
typedef __bf16 bf16x8 __attribute__((ext_vector_type(8)));
typedef __bf16 bf16x2 __attribute__((ext_vector_type(2)));
typedef __amdgpu_buffer_rsrc_t rsrc_t;
#define LAS __attribute__((address_space(3)))
#define GAS __attribute__((address_space(1)))
#define DEV __device__ __forceinline__

constexpr int S_ = 2048, DM = 448, NH = 16, DH = 256, FF = 1792, NSEG = 64;
constexpr unsigned MB = 1u << 20;
constexpr unsigned OFF_HO = 0;
constexpr unsigned OFF_CTL = 4 * MB;
constexpr unsigned CTL_KV = OFF_CTL;
constexpr unsigned CTL_H = OFF_CTL + 8192;
constexpr unsigned CTL_E = OFF_CTL + 16384;
constexpr unsigned CTL_POL = OFF_CTL + 32768;
constexpr unsigned CTL_BYTES = 32768;
constexpr unsigned OFF_DBG = 100 * MB;
constexpr unsigned OFF_SXM = 5 * MB;
constexpr unsigned OFF_MP = 7 * MB;
constexpr unsigned FILL2_BYTES = 9 * MB;
constexpr unsigned OFF_XF = 14 * MB;
constexpr unsigned OFF_WD = 16 * MB;
constexpr unsigned OFF_EX = 18 * MB;
constexpr unsigned OFF_LX = 20 * MB;
constexpr unsigned OFF_HF = 21 * MB;
constexpr unsigned OFF_WF = 28 * MB;
constexpr unsigned OFF_KX = 44 * MB;
constexpr unsigned OFF_DX = 72 * MB;
constexpr unsigned WS_USED = 102 * MB;
constexpr unsigned SENT = 0xFFFFFFFFu;
constexpr unsigned SPIN_CAP = 1u << 19;
constexpr int POLL_BUDGET = 1 << 20;
constexpr int LDS_BYTES = 131072;

DEV unsigned pack2(float lo, float hi) { f32x2 f = {lo, hi}; return __builtin_bit_cast(unsigned, __builtin_convertvector(f, bf16x2)); }
DEV v4u pack8(f32x4 a, f32x4 b) { return (v4u){pack2(a[0], a[1]), pack2(a[2], a[3]), pack2(b[0], b[1]), pack2(b[2], b[3])}; }
DEV unsigned fixs(unsigned x) { return x < 0xFFFFFFFEu ? x : 0xFFFFFFFEu; }
DEV v4u fixs4(v4u v) { return (v4u){fixs(v.x), fixs(v.y), fixs(v.z), fixs(v.w)}; }
DEV float bflo(unsigned u) { return __builtin_bit_cast(float, u << 16); }
DEV float bfhi(unsigned u) { return __builtin_bit_cast(float, u & 0xFFFF0000u); }
DEV f32x4 mma(v4u a, v4u b, f32x4 c) { return __builtin_amdgcn_mfma_f32_16x16x32_bf16(__builtin_bit_cast(bf16x8, a), __builtin_bit_cast(bf16x8, b), c, 0, 0, 0); }
DEV v4u ld16c(rsrc_t r, unsigned so, unsigned vo) { return __builtin_amdgcn_raw_buffer_load_b128(r, vo, so, 16); }
DEV unsigned ld4c(rsrc_t r, unsigned so, unsigned vo) { return __builtin_amdgcn_raw_buffer_load_b32(r, vo, so, 16); }
DEV v4u ld16p(rsrc_t r, unsigned so, unsigned vo) { return __builtin_amdgcn_raw_buffer_load_b128(r, vo, so, 0); }
DEV void st16c(rsrc_t r, unsigned so, unsigned vo, v4u v) { __builtin_amdgcn_raw_buffer_store_b128(v, r, vo + so, 0, 16); }
DEV void st8c(rsrc_t r, unsigned so, unsigned vo, v2u v) { __builtin_amdgcn_raw_buffer_store_b64(v, r, vo, so, 16); }
DEV void st4c(rsrc_t r, unsigned so, unsigned vo, unsigned v) { __builtin_amdgcn_raw_buffer_store_b32(v, r, vo, so, 16); }
DEV void st16p(rsrc_t r, unsigned so, unsigned vo, v4u v) { __builtin_amdgcn_raw_buffer_store_b128(v, r, vo + so, 0, 0); }
DEV unsigned umax(unsigned a, unsigned b) { return a > b ? a : b; }
DEV unsigned max4u(v4u v) { return umax(umax(v.x, v.y), umax(v.z, v.w)); }
DEV unsigned ror8(unsigned x) { return __builtin_amdgcn_mov_dpp(x, 0x128, 0xf, 0xf, false); }
DEV float ror8f(float x) { return __builtin_bit_cast(float, ror8(__builtin_bit_cast(unsigned, x))); }
DEV v4u ror8v(v4u v) { return (v4u){ror8(v.x), ror8(v.y), ror8(v.z), ror8(v.w)}; }
DEV float x16max(float x) { unsigned u = __builtin_bit_cast(unsigned, x); auto r = __builtin_amdgcn_permlane16_swap(u, u, false, false); return fmaxf(__builtin_bit_cast(float, (unsigned)r[0]), __builtin_bit_cast(float, (unsigned)r[1])); }
DEV float x32max(float x) { unsigned u = __builtin_bit_cast(unsigned, x); auto r = __builtin_amdgcn_permlane32_swap(u, u, false, false); return fmaxf(__builtin_bit_cast(float, (unsigned)r[0]), __builtin_bit_cast(float, (unsigned)r[1])); }
DEV float x16add(float x) { unsigned u = __builtin_bit_cast(unsigned, x); auto r = __builtin_amdgcn_permlane16_swap(u, u, false, false); return __builtin_bit_cast(float, (unsigned)r[0]) + __builtin_bit_cast(float, (unsigned)r[1]); }
DEV float x32add(float x) { unsigned u = __builtin_bit_cast(unsigned, x); auto r = __builtin_amdgcn_permlane32_swap(u, u, false, false); return __builtin_bit_cast(float, (unsigned)r[0]) + __builtin_bit_cast(float, (unsigned)r[1]); }
DEV float ex2(float x) { return __builtin_amdgcn_exp2f(x); }
DEV float lg2(float x) { return __builtin_amdgcn_logf(x); }
DEV float rcp(float x) { return __builtin_amdgcn_rcpf(x); }
#define CFENCE() asm volatile("" ::: "memory")
#define WG_BAR() do { asm volatile("s_waitcnt lgkmcnt(0)" ::: "memory"); __builtin_amdgcn_s_barrier(); asm volatile("" ::: "memory"); } while (0)
#define WAIT_VM(n) asm volatile("s_waitcnt vmcnt(" #n ")" ::: "memory")
DEV v4u lds16(LAS char* l, unsigned off) { return *(LAS v4u*)(l + off); }
DEV v2u lds8(LAS char* l, unsigned off) { return *(LAS v2u*)(l + off); }
DEV f32x4 lds16f(LAS char* l, unsigned off) { return *(LAS f32x4*)(l + off); }
DEV f32x2 lds8f(LAS char* l, unsigned off) { return *(LAS f32x2*)(l + off); }
DEV v4u onehot(int g, int want) {
  v4u r; const int i = want - 8 * g;
#pragma unroll
  for (int q = 0; q < 4; ++q) r[q] = (i == 2 * q) ? 0x3F80u : ((i == 2 * q + 1) ? 0x3F800000u : 0u);
  return r;
}
DEV unsigned ho_blk(int s, int type, unsigned pol0, unsigned pol1) { return OFF_HO + (unsigned)(s >> 5) * (2 * MB) + (unsigned)((s & 31) * 3 + type) * 16384u + ((s >> 5) ? pol1 : pol0); }

#define ST_DECL do {} while (0)
#define ST_AT(k) do {} while (0)
#define ST_END(s) do {} while (0)
#define ST_DUMP() do {} while (0)

#define DMA1(o, c) "buffer_load_dwordx4 %1, %2, %3 offen" o c " lds\n\t"
#define DMA_ASM(body) asm volatile("s_mov_b32 m0, %0\n\ts_nop 3\n\t" body :: "s"(dst), "v"(vo), "s"(R), "s"(so) : "memory")
template <int N, bool COH> DEV void dma(rsrc_t R, unsigned dst, unsigned so, unsigned vo) {
  if constexpr (COH) {
    if constexpr (N == 1) DMA_ASM(DMA1("", " sc1"));
    else if constexpr (N == 3) DMA_ASM(DMA1("", " sc1") DMA1(" offset:1024", " sc1") DMA1(" offset:2048", " sc1"));
    else DMA_ASM(DMA1("", " sc1") DMA1(" offset:1024", " sc1") DMA1(" offset:2048", " sc1") DMA1(" offset:3072", " sc1"));
  } else {
    if constexpr (N == 1) DMA_ASM(DMA1("", ""));
    else if constexpr (N == 3) DMA_ASM(DMA1("", "") DMA1(" offset:1024", "") DMA1(" offset:2048", ""));
    else DMA_ASM(DMA1("", "") DMA1(" offset:1024", "") DMA1(" offset:2048", "") DMA1(" offset:3072", ""));
  }
}

#define SB() __builtin_amdgcn_sched_barrier(0)
#define POLL_HEAD "s_branch 0f\n4:\n\ts_waitcnt vmcnt(0)\n\ts_branch 3f\n0:\n\t"
#define POLL_BUD(b) "s_sub_u32 " b ", " b ", 1\n\ts_cmp_lt_i32 " b ", 0\n\ts_cbranch_scc1 4b\n\t"
DEV v4u poll16(rsrc_t R, unsigned so, unsigned vo, int& bud) {
  v4u d; unsigned t;
  asm volatile(POLL_HEAD
    "buffer_load_dwordx4 v[232:235], %3, %4, %5 offen sc1\n\t" POLL_BUD("%2")
    "s_waitcnt vmcnt(0)\n\t"
    "v_max_u32 %1, v234, v235\n\tv_max3_u32 %1, v232, v233, %1\n\tv_cmp_eq_u32 vcc, -1, %1\n\ts_cbranch_vccnz 0b\n3:"
    : "=&{v[232:235]}"(d), "=&v"(t), "+s"(bud) : "v"(vo), "s"(R), "s"(so) : "memory", "vcc", "scc");
  return d;
}
DEV void wait_cnt(rsrc_t R, unsigned base, int lane, int nlanes, unsigned& cnt, int idx, unsigned target, unsigned& cap, bool nap) {
  for (unsigned n = 0;; ++n) {
    if ((unsigned)__builtin_amdgcn_readlane(cnt, idx) >= target) break;
    if (n >= cap) { cap = 0; break; }
    if (nap) __builtin_amdgcn_s_sleep(8);
    CFENCE();
    cnt = ld4c(R, base, (unsigned)(lane & (nlanes - 1)) * 256u);
  }
  CFENCE();
}
DEV void signal(char* wsb, unsigned off) { __hip_atomic_fetch_add((GAS unsigned*)(wsb + off), 1u, __ATOMIC_RELAXED, __HIP_MEMORY_SCOPE_AGENT); }

struct ProArgs { const unsigned short *x, *mem0, *wq, *wk, *wv, *wo, *wg, *wu, *wd; char* ws; };
DEV char* ws_base(char* ws) { return (char*)(((size_t)ws + 2 * MB - 1) & ~(size_t)(2 * MB - 1)); }
DEV v4u gl16(const unsigned short* p) { return *(const GAS v4u*)p; }

constexpr int PRO_WG_W = 224, PRO_WG_T = 42, PRO_WG_L = 32, PRO_GRID = PRO_WG_W + PRO_WG_T + PRO_WG_L + 1;

__global__ void __launch_bounds__(512, 4) pro_kernel(ProArgs a) {
  const int b = blockIdx.x, w = __builtin_amdgcn_readfirstlane(threadIdx.x >> 6), lane = threadIdx.x & 63, g = lane >> 4, rho = lane & 15;
  char* const wsb = ws_base(a.ws);
  const rsrc_t R = __builtin_amdgcn_make_buffer_rsrc(wsb, 0, WS_USED, 0x00020000);
  const int prow = 8 * (rho >> 2) + (rho & 3);
  if (b < PRO_WG_W) {
    if (w == 7) {
      const v4u sv = {SENT, SENT, SENT, SENT};
      for (unsigned c = b; c < 12288u; c += PRO_WG_W) {
        unsigned off;
        if (c < 1536u) { if (c < 16u) continue; off = OFF_HO + c * 1024u; }
        else if (c < 3072u) off = OFF_HO + 2 * MB + (c - 1536u) * 1024u;
        else off = OFF_SXM + (c - 3072u) * 1024u;
        st16p(R, off, lane * 16, sv);
      }
      return;
    }
    const int isv = b >= 112, bb = isv ? b - 112 : b, h = bb / 7, cb = bb % 7;
    const unsigned short* wa = (isv ? a.wv : a.wk) + (size_t)(64 * w + prow) * 4096 + h * 256 + 8 * g;
    f32x4 acc[4][4];
#pragma unroll
    for (int i = 0; i < 4; ++i)
#pragma unroll
      for (int j = 0; j < 4; ++j) acc[i][j] = (f32x4){0.f, 0.f, 0.f, 0.f};
    const v4u sel0 = onehot(g, rho), sel1 = onehot(g, 16 + rho);
#pragma unroll 1
    for (int es = 0; es < 8; ++es) {
      v4u A[4], B[4];
#pragma unroll
      for (int i = 0; i < 4; ++i) A[i] = gl16(wa + (size_t)(32 * (i >> 1) + 4 * (i & 1)) * 4096 + 32 * es);
      if (!isv) {
#pragma unroll
        for (int j = 0; j < 4; ++j) B[j] = gl16(a.wq + (size_t)(64 * cb + 32 * (j >> 1) + 4 * (j & 1) + prow) * 4096 + h * 256 + 32 * es + 8 * g);
      } else {
#pragma unroll
        for (int cc = 0; cc < 2; ++cc) {
          const unsigned short* p = a.wo + (size_t)(h * 256 + 32 * es + prow) * DM + 64 * cb + 32 * cc + 8 * g;
          const v4u n0 = gl16(p), n1 = gl16(p + 4 * DM);
          const f32x4 z = {0.f, 0.f, 0.f, 0.f};
          B[2 * cc] = pack8(mma(n0, sel0, z), mma(n1, sel0, z));
          B[2 * cc + 1] = pack8(mma(n0, sel1, z), mma(n1, sel1, z));
        }
      }
#pragma unroll
      for (int i = 0; i < 4; ++i)
#pragma unroll
        for (int j = 0; j < 4; ++j) acc[i][j] = mma(A[i], B[j], acc[i][j]);
    }
    const float sc = isv ? 1.f : 0.0625f * 1.4426950408889634f;
#pragma unroll
    for (int j = 0; j < 4; ++j)
#pragma unroll
      for (int u = 0; u < 2; ++u)
        st16p(R, OFF_WF + (unsigned)((isv * 448 + h * 28 + 4 * cb + j) * 14 + 2 * w + u) * 1024u, lane * 16, pack8(acc[2 * u][j] * sc, acc[2 * u + 1][j] * sc));
  } else if (b < PRO_WG_W + PRO_WG_T) {
    const int wv = (b - PRO_WG_W) * 8 + w, mat = wv / 112, r0 = (wv % 112) * 7;
    const v4u sel0 = onehot(g, rho), sel1 = onehot(g, 16 + rho);
    v4u n0[7], n1[7];
#pragma unroll
    for (int i = 0; i < 7; ++i) {
      const int blk = r0 + i;
      const unsigned short* p;
      int ld;
      if (mat < 2) { const int kb = blk / 56, fb = blk % 56; ld = FF; p = (mat ? a.wu : a.wg) + (size_t)(32 * kb + prow) * FF + 32 * fb + 8 * g; }
      else { const int kb = blk / 14, cb = blk % 14; ld = DM; p = a.wd + (size_t)(32 * kb + prow) * DM + 32 * cb + 8 * g; }
      n0[i] = gl16(p); n1[i] = gl16(p + 4 * ld);
    }
#pragma unroll
    for (int i = 0; i < 7; ++i) {
      const int blk = r0 + i;
      const f32x4 z = {0.f, 0.f, 0.f, 0.f};
      const v4u f0 = pack8(mma(n0[i], sel0, z), mma(n1[i], sel0, z)), f1 = pack8(mma(n0[i], sel1, z), mma(n1[i], sel1, z));
      unsigned o0, o1;
      if (mat < 2) { const int kb = blk / 56, fb = blk % 56; o0 = OFF_WF + (unsigned)((896 + 4 * fb + mat) * 14 + kb) * 1024u; o1 = o0 + 2 * 14 * 1024u; }
      else { const int kb = blk / 14, cb = blk % 14; o0 = OFF_WD + (unsigned)(2 * cb * 56 + kb) * 1024u; o1 = o0 + 56 * 1024u; }
      st16p(R, o0, lane * 16, f0); st16p(R, o1, lane * 16, f1);
    }
  } else if (b < PRO_WG_W + PRO_WG_T + PRO_WG_L) {
    const int row0 = ((b - PRO_WG_W - PRO_WG_T) * 8 + w) * 8;
    const bool act = lane < 56;
    v4u v[8];
#pragma unroll
    for (int i = 0; i < 8; ++i) v[i] = act ? gl16(a.x + (size_t)(row0 + i) * DM + lane * 8) : (v4u){0u, 0u, 0u, 0u};
#pragma unroll
    for (int i = 0; i < 8; ++i) {
      float f[8];
#pragma unroll
      for (int q = 0; q < 4; ++q) { f[2 * q] = bflo(v[i][q]); f[2 * q + 1] = bfhi(v[i][q]); }
      float s = 0.f;
#pragma unroll
      for (int q = 0; q < 8; ++q) s += f[q];
#pragma unroll
      for (int o = 1; o < 64; o <<= 1) s += __shfl_xor(s, o);
      const float mean = s * (1.f / DM);
      float vs = 0.f;
#pragma unroll
      for (int q = 0; q < 8; ++q) { f[q] = act ? f[q] - mean : 0.f; vs += f[q] * f[q]; }
#pragma unroll
      for (int o = 1; o < 64; o <<= 1) vs += __shfl_xor(vs, o);
      const float rs = 1.f / sqrtf(vs * (1.f / DM) + 1e-5f);
      const int row = row0 + i;
      const v4u o4 = {pack2(f[0] * rs, f[1] * rs), pack2(f[2] * rs, f[3] * rs), pack2(f[4] * rs, f[5] * rs), pack2(f[6] * rs, f[7] * rs)};
      if (act) st16p(R, OFF_XF + (unsigned)((row >> 4) * 14) * 1024u + (unsigned)(row & 15) * 16u, (unsigned)(lane >> 2) * 1024u + (unsigned)(lane & 3) * 256u, o4);
    }
  } else {
    if (w < 2) {
      unsigned best[2] = {~0u, ~0u};
      for (int i = 0; i < 16; ++i) {
        const char* p = wsb + OFF_HO + (size_t)w * (2 * MB) + (size_t)(100 + (i >> 1)) * 16384 + (i & 1) * 8192 + 1024;
        unsigned v;
        const unsigned long long c0 = __builtin_amdgcn_s_memtime();
        asm volatile("s_waitcnt lgkmcnt(0)\n\tglobal_load_dword %0, %1, off sc1\n\ts_waitcnt vmcnt(0)" : "=&v"(v) : "v"(p) : "memory");
        const unsigned long long c1 = __builtin_amdgcn_s_memtime();
        const unsigned d = (unsigned)(c1 - c0);
        if (i & 1) best[1] = d < best[1] ? d : best[1]; else best[0] = d < best[0] ? d : best[0];
      }
      if (lane == 0) __builtin_amdgcn_raw_buffer_store_b32(best[1] < best[0] ? 8192u : 0u, R, CTL_POL + w * 4, 0, 0);
    } else if (w == 2) {
      for (unsigned c = 0; c < CTL_BYTES / 1024; ++c) st16p(R, OFF_CTL + c * 1024u, lane * 16, (v4u){0u, 0u, 0u, 0u});
    } else if (w == 3) {
      for (int c = 0; c < 8; ++c) {
        v4u v = {SENT, SENT, SENT, SENT};
        if (c < 7) { const int pc = c * 64 + lane; v = fixs4(gl16(a.mem0 + (size_t)(pc & 7) * DM + (pc >> 3) * 8)); }
        st16p(R, OFF_HO + c * 1024u, lane * 16, v);
        st16p(R, OFF_HO + 8192u + c * 1024u, lane * 16, v);
      }
    }
  }
}

struct MegaArgs { const unsigned short* x; unsigned short* out; char* ws; };

struct Fetch {
  unsigned rdy, nxt, T; unsigned long long marks;
  DEV void refresh(rsrc_t R, int lane) { const unsigned c = ld4c(R, CTL_KV, (unsigned)(lane & 31) * 256u); rdy = (unsigned)__builtin_ctzll(~(__ballot(c >= 112u) & 0xFFFFFFFFull)); }
  DEV bool go(rsrc_t R, int lane, unsigned must, unsigned lim, bool first, unsigned& cap) {
    if (nxt > lim) return false;
    if ((nxt >> 1) >= rdy && first) {
      refresh(R, lane);
      for (unsigned n = 0; nxt <= must && (nxt >> 1) >= rdy; ++n) { if (n >= cap) { cap = 0; rdy = 32; break; } CFENCE(); refresh(R, lane); }
    }
    return (nxt >> 1) < rdy;
  }
  DEV void issued(unsigned n) { T += n; const unsigned sh = (nxt & 3u) * 16u; marks = (marks & ~(0xFFFFull << sh)) | ((unsigned long long)T << sh); ++nxt; }
  DEV unsigned since(unsigned seg) const { return T - (unsigned)((marks >> ((seg & 3u) * 16u)) & 0xFFFFull); }
};

#define MFMA "v_mfma_f32_16x16x32_bf16 "
#define CVT "v_cvt_pk_bf16_f32 "
#define ROR8 " row_ror:8 row_mask:0xf bank_mask:0xf\n\t"

DEV void role_v(rsrc_t R, LAS char* L, int v, int w, int lane, unsigned pol0, unsigned pol1) {
  const int g = lane >> 4, rho = lane & 15, hd = rho >> 3, r = rho & 7;
  constexpr unsigned L_M = 0, L_MP = 7168, L_RED = 11264, L_FLAG = 13312, L_D = 16384;
  const unsigned lb = (unsigned)(size_t)L;
  const int pr = w < 4 ? 2 * w : 2 * w - 7;
  ST_DECL;
  v4u C[2][14];
#pragma unroll
  for (int a = 0; a < 2; ++a)
#pragma unroll
    for (int k = 0; k < 14; ++k) C[a][k] = ld16p(R, OFF_WF + (unsigned)((448 + (2 * pr + a) * 28 + v) * 14 + k) * 1024u, lane * 16);
  const unsigned mrd = L_M + (unsigned)(g * 128 + r * 16);
  auto mprime = [&]() {
    v4u mf[14];
#pragma unroll
    for (int k = 0; k < 14; ++k) mf[k] = lds16(L, mrd + k * 512);
    SB();
    f32x4 acc0 = {0.f, 0.f, 0.f, 0.f}, acc1 = acc0;
#pragma unroll
    for (int k = 0; k < 14; ++k) { acc0 = mma(mf[k], C[0][k], acc0); acc1 = mma(mf[k], C[1][k], acc1); }
    return pack8(acc0, acc1); };
  auto last = [&](v4u mp) { if (g < 2) st16c(R, OFF_MP + (unsigned)(((NSEG - 1) * 8 + pr) * 28 + v) * 512u, lane * 16, fixs4(mp)); };
  if (w < 4) {
    int bud = POLL_BUDGET;
    const v4u ks1 = onehot(0, r), ks2 = onehot(0, r ^ 4), ones = {0x3F803F80u, 0x3F803F80u, 0x3F803F80u, 0x3F803F80u};
    v4u sel;
    if (g < 2) sel = hd ? (v4u){0x0C0C0C0Cu, 0x0C0C0C0Cu, 0x03020100u, 0x07060504u} : (v4u){0x03020100u, 0x07060504u, 0x0C0C0C0Cu, 0x0C0C0C0Cu};
    else { const v4u o = onehot(0, ((r >> 2) == g - 2) ? 4 * hd + (r & 3) : -1);
#pragma unroll
      for (int q = 0; q < 4; ++q) sel[q] = (o[q] & 0xFFFFu) ? 0x0C0C0100u : (o[q] ? 0x01000C0Cu : 0x0C0C0C0Cu); }
    const unsigned red = lb + L_RED + (unsigned)(w * 512 + (g >> 1) * 256 + r * 32 + (g & 1) * 16);
    const unsigned eoff = (unsigned)((1 - hd) * 512 + (g * 8 + r) * 16), loff = (unsigned)(hd * 64 + r * 4);
    const unsigned m2 = w < 3 ? 1024u : 0u;
    __builtin_amdgcn_s_setprio(2);
    __builtin_amdgcn_s_waitcnt(0x0F70);
    WG_BAR();
    for (int s = 0;; ++s) {
      const unsigned bm = ho_blk(s, 0, pol0, pol1);
      v4u D[4];
#pragma unroll
      for (int i = 0; i < 4; ++i) D[i] = lds16(L, L_D + (s & 3) * 16384 + (4 * w + i) * 1024 + lane * 16);
      { unsigned t;
        asm volatile(POLL_HEAD
          "buffer_load_dwordx4 v[232:235], %2, %3, %4 offen sc1\n\tbuffer_load_dwordx4 v[236:239], %2, %3, %5 offen sc1\n\t" POLL_BUD("%1")
          "s_waitcnt vmcnt(1)\n\tds_write_b128 %6, v[232:235]\n\tv_max3_u32 %0, v232, v233, v234\n\ts_waitcnt vmcnt(0)\n\tds_write_b128 %7, v[236:239]\n\t"
          "v_max3_u32 %0, %0, v235, v236\n\tv_max3_u32 %0, %0, v237, v238\n\tv_max_u32 %0, %0, v239\n\tv_cmp_eq_u32 vcc, -1, %0\n\ts_cbranch_vccnz 0b\n3:"
          : "=&v"(t), "+s"(bud)
          : "v"(lane * 16), "s"(R), "s"(bm + w * 2048), "s"(bm + w * 2048 + m2), "v"(lb + L_M + w * 2048 + lane * 16), "v"(lb + L_M + w * 2048 + m2 + lane * 16)
          : "memory", "vcc", "scc", "v232", "v233", "v234", "v235", "v236", "v237", "v238", "v239"); }
      ST_AT(0);
      WG_BAR();
      v4u za[2], ef[2], sp[2]; unsigned lse[2];
      za[0] = mprime();
      if (s == NSEG - 1) { last(za[0]); break; }
      if (g < 2) *(LAS v4u*)(L + L_MP + pr * 512 + lane * 16) = za[0];
      ST_AT(1);
      const unsigned so = ho_blk(s, 1, pol0, pol1) + (unsigned)w * 2048u;
      { unsigned t;
        asm volatile(POLL_HEAD
          "buffer_load_dwordx4 v[232:235], %8, %10, %11 offen sc1\n\tbuffer_load_dwordx4 v[236:239], %8, %10, %11 offen offset:1024 sc1\n\t"
          "buffer_load_dword %2, %9, %10, %12 offen sc1\n\tbuffer_load_dword %3, %9, %10, %12 offen offset:128 sc1\n\t"
          "buffer_load_dwordx4 v[240:243], %13, %10, %14 offen sc1\n\tbuffer_load_dwordx4 v[244:247], %13, %10, %14 offen offset:1024 sc1\n\t" POLL_BUD("%5")
          "s_waitcnt vmcnt(5)\n\tv_max3_u32 %4, v232, v233, v234\n\ts_waitcnt vmcnt(4)\n\tv_max3_u32 %4, %4, v235, v236\n\tv_max3_u32 %4, %4, v237, v238\n\t"
          "s_waitcnt vmcnt(2)\n\tv_max3_u32 %4, %4, v239, %2\n\tv_max_u32 %4, %4, %3\n\tv_cmp_eq_u32 vcc, -1, %4\n\ts_cbranch_vccnz 0b\n3:\n\t"
          "buffer_load_dwordx4 v[240:243], %13, %10, %14 offen sc1\n\tbuffer_load_dwordx4 v[244:247], %13, %10, %14 offen offset:1024 sc1"
          : "=&{v[232:235]}"(ef[0]), "=&{v[236:239]}"(ef[1]), "=&v"(lse[0]), "=&v"(lse[1]), "=&v"(t), "+s"(bud), "=&{v[240:243]}"(sp[0]), "=&{v[244:247]}"(sp[1])
          : "v"(eoff), "v"(loff), "s"(R), "s"(ho_blk(s, 2, pol0, pol1) + (unsigned)w * 2048u), "s"(bm + 7168u + (unsigned)w * 256u), "v"(lane * 16), "s"(so) : "memory", "vcc", "scc"); }
      ST_AT(2);
      for (;;) {
        const unsigned f = *(LAS unsigned*)(L + L_FLAG + w * 4);
        CFENCE();
        za[1] = lds16(L, L_MP + (pr + 1) * 512 + (lane & 31) * 16);
        CFENCE();
        if (__builtin_expect(__builtin_amdgcn_readfirstlane(f) == (unsigned)s + 1u, 1)) break;
      }
#pragma unroll
      for (int a = 0; a < 2; ++a) {
        const f32x4 z = {0.f, 0.f, 0.f, 0.f};
        const v4u y = pack8(mma(ef[a], D[2 * a], z), mma(ror8v(ef[a]), D[2 * a + 1], z));
#pragma unroll
        for (int q = 0; q < 4; ++q) za[a][q] = g < 2 ? za[a][q] : y[q];
      }
      asm volatile("s_branch 1f\n4:\n\ts_waitcnt vmcnt(0)\n\ts_branch 3f\n0:\n\t"
        "buffer_load_dwordx4 v[240:243], %[vo], %[R], %[so] offen sc1\n\tbuffer_load_dwordx4 v[244:247], %[vo], %[R], %[so] offen offset:1024 sc1\n\t" POLL_BUD("%[bud]")
        "1:\n\ts_waitcnt vmcnt(2)\n\t"
        "v_max3_u32 v216, v240, v241, v242\n\t"
        "v_max3_u32 v217, v243, v244, v245\n\t"
        "v_max3_u32 v216, v216, v246, v247\n\t"
        MFMA "v[200:203], %[k1], v[240:243], 0\n\t"
        "v_max_u32 v216, v216, v217\n\t"
        MFMA "v[204:207], %[k2], v[240:243], 0\n\t"
        "v_cmp_eq_u32 vcc, -1, v216\n\t"
        "s_cbranch_vccnz 0b\n3:\n\t"
        MFMA "v[208:211], %[k1], v[244:247], 0\n\t"
        MFMA "v[212:215], %[k2], v[244:247], 0\n\t"
        "s_nop 1\n\t"
        "v_max3_f32 v216, v200, v201, v202\n\t"
        "v_cndmask_b32 v200, %[l0], v200, %[gm]\n\t"
        "v_max3_f32 v217, v203, v204, v205\n\t"
        "v_max3_f32 v218, v206, v207, %[l0]\n\t"
        "s_nop 0\n\t"
        "v_max3_f32 v216, v216, v217, v218\n\t"
        "v_max3_f32 v219, v208, v209, v210\n\t"
        "v_sub_f32 v200, v200, v216\n\tv_sub_f32 v201, v201, v216\n\tv_sub_f32 v202, v202, v216\n\tv_sub_f32 v203, v203, v216\n\t"
        "v_exp_f32 v200, v200\n\tv_exp_f32 v201, v201\n\tv_exp_f32 v202, v202\n\tv_exp_f32 v203, v203\n\t"
        "v_max3_f32 v220, v211, v212, v213\n\t"
        CVT "v217, v200, v201\n\t" CVT "v218, v202, v203\n\t"
        "v_max3_f32 v221, v214, v215, %[l1]\n\t"
        "v_perm_b32 v204, v218, v217, %[s0]\n\tv_perm_b32 v205, v218, v217, %[s1]\n\tv_perm_b32 v206, v218, v217, %[s2]\n\tv_perm_b32 v207, v218, v217, %[s3]\n\t"
        "v_cndmask_b32 v208, %[l1], v208, %[gm]\n\t"
        "v_max3_f32 v219, v219, v220, v221\n\t"
        MFMA "v[200:203], %[on], v[204:207], 0\n\t"
        "v_sub_f32 v208, v208, v219\n\tv_sub_f32 v209, v209, v219\n\t"
        MFMA "v[224:227], %[z0], v[204:207], 0\n\t"
        "v_sub_f32 v210, v210, v219\n\tv_sub_f32 v211, v211, v219\n\t"
        "v_exp_f32 v208, v208\n\tv_exp_f32 v209, v209\n\tv_exp_f32 v210, v210\n\tv_exp_f32 v211, v211\n\t"
        "v_rcp_f32 v216, v200\n\t"
        CVT "v217, v208, v209\n\t" CVT "v218, v210, v211\n\t"
        "v_mul_f32 v224, v224, v216\n\t"
        "v_perm_b32 v212, v218, v217, %[s0]\n\tv_perm_b32 v213, v218, v217, %[s1]\n\tv_perm_b32 v214, v218, v217, %[s2]\n\tv_perm_b32 v215, v218, v217, %[s3]\n\t"
        "v_mul_f32 v225, v225, v216\n\tv_mul_f32 v226, v226, v216\n\t"
        MFMA "v[208:211], %[on], v[212:215], 0\n\t"
        "v_mul_f32 v227, v227, v216\n\t"
        MFMA "v[228:231], %[z1], v[212:215], 0\n\t"
        "s_nop 5\n\t"
        "v_rcp_f32 v216, v208\n\t"
        "s_nop 0\n\t"
        "v_fma_f32 v224, v228, v216, v224\n\tv_fma_f32 v225, v229, v216, v225\n\tv_fma_f32 v226, v230, v216, v226\n\tv_fma_f32 v227, v231, v216, v227\n\t"
        "v_add_f32_dpp v224, v224, v224" ROR8 "v_add_f32_dpp v225, v225, v225" ROR8 "v_add_f32_dpp v226, v226, v226" ROR8 "v_add_f32_dpp v227, v227, v227" ROR8
        "ds_write_b128 %[red], v[224:227]\n\ts_waitcnt lgkmcnt(0)\n\ts_barrier\n\ts_waitcnt vmcnt(0)"
        : [bud] "+s"(bud), "+{v[240:243]}"(sp[0]), "+{v[244:247]}"(sp[1])
        : [vo] "v"(lane * 16), [R] "s"(R), [so] "s"(so), [k1] "v"(ks1), [k2] "v"(ks2), [on] "v"(ones), [z0] "v"(za[0]), [z1] "v"(za[1]),
          [l0] "v"(lse[0]), [l1] "v"(lse[1]), [gm] "s"(0xFFFFFFFFull), [s0] "v"(sel.x), [s1] "v"(sel.y), [s2] "v"(sel.z), [s3] "v"(sel.w), [red] "v"(red)
        : "memory", "vcc", "scc", "v200", "v201", "v202", "v203", "v204", "v205", "v206", "v207", "v208", "v209", "v210", "v211", "v212", "v213", "v214", "v215",
          "v216", "v217", "v218", "v219", "v220", "v221", "v224", "v225", "v226", "v227", "v228", "v229", "v230", "v231");
      ST_AT(3);
      ST_END(s);
    }
  } else {
    unsigned cap = SPIN_CAP;
    Fetch F = {0u, 0u, 0u, 0ull};
    auto fetch = [&](unsigned must, unsigned lim) {
      for (int i = 0; i < 2 && F.go(R, lane, must, lim, i == 0, cap); ++i) {
#pragma unroll
        for (int h = 0; h < 8; ++h) dma<1, true>(R, lb + L_D + (F.nxt & 3u) * 16384u + ((w - 4) * 8 + h) * 1024, OFF_DX + (F.nxt * 448u + ((w - 4) * 8 + h) * 28 + v) * 1024u, lane * 16);
        F.issued(8);
      } };
    *(LAS unsigned*)(L + L_FLAG + (w - 4) * 4) = 0u;
    if (w < 6) fetch(0, 1);
    __builtin_amdgcn_s_waitcnt(0x0F70);
    WG_BAR();
    for (int s = 0;; ++s) {
      WG_BAR();
      const v4u mp = mprime();
      if (s == NSEG - 1) { last(mp); break; }
      if (g < 2) *(LAS v4u*)(L + L_MP + pr * 512 + lane * 16) = mp;
      CFENCE();
      *(LAS unsigned*)(L + L_FLAG + (w - 4) * 4) = (unsigned)s + 1u;
      ST_AT(1);
      if (w < 6) {
        fetch(s + 1, s + 3 < NSEG - 2 ? s + 3 : NSEG - 2);
        const unsigned n = F.since(s + 1);
        if (n >= 16u) WAIT_VM(16); else if (n >= 8u) WAIT_VM(8); else WAIT_VM(0);
      }
      ST_AT(2);
      WG_BAR();
      if (w == 7) {
        f32x2 t[4];
#pragma unroll
        for (int i = 0; i < 4; ++i) t[i] = lds8f(L, L_RED + i * 512 + lane * 8);
        const f32x2 u = (t[0] + t[1]) + (t[2] + t[3]);
        st4c(R, ho_blk(s + 1, 0, pol0, pol1) + v * 256, lane * 4, fixs(pack2(u[0], u[1])));
      } else if (w == 6) {
#pragma unroll
        for (int i = 0; i < 4; ++i) st16c(R, OFF_MP + (unsigned)((s * 8 + 2 * i) * 28 + v) * 512u, (lane >> 5) * (28 * 512) + (lane & 31) * 16, fixs4(lds16(L, L_MP + i * 1024 + lane * 16)));
      }
      ST_AT(3);
      ST_END(s);
    }
  }
  ST_DUMP();
}

DEV void role_q(rsrc_t R, LAS char* L, int h, int p, int w, int lane, unsigned pol0, unsigned pol1) {
  const int g = lane >> 4, rho = lane & 15, hd = rho >> 3, r = rho & 7;
  constexpr unsigned L_M = 0, L_RED = 7168, L_RED2 = 9216, L_X = 16384, L_DUMP = 49152;
  const unsigned lb = (unsigned)(size_t)L;
  ST_DECL;
  if (w < 7) {
    int bud = POLL_BUDGET;
    v4u A[7][2];
#pragma unroll
    for (int t = 0; t < 7; ++t)
#pragma unroll
      for (int u = 0; u < 2; ++u) A[t][u] = ld16p(R, OFF_WF + (unsigned)((h * 28 + (t < 6 ? 6 * p + t : 24 + p)) * 14 + 2 * w + u) * 1024u, lane * 16);
    const unsigned mo = (unsigned)(((2 * w + hd) * 4 + g) * 128 + r * 16);
    const unsigned mb = lb + L_M + (unsigned)((3 * p * 4 + g) * 128 + r * 16), mh = lb + L_M + (unsigned)(((12 + (p >> 1)) * 4 + g) * 128 + r * 16 + 8 * (p & 1));
    const unsigned red = lb + (g < 2 ? L_RED + (unsigned)(w * 256 + r * 32 + g * 16) : L_DUMP + (unsigned)(w * 1024 + lane * 16));
    v4u k0, k1, k2, kh = {0u, 0u, 0u, 0u}, bh = {0u, 0u, 0u, 0u};
    __builtin_amdgcn_s_waitcnt(0x0F70);
    for (int s = 0; s < NSEG; ++s) {
      asm volatile(POLL_HEAD
        "buffer_load_dwordx4 v[232:235], %[mo], %[R], %[so] offen sc1\n\t" POLL_BUD("%[bud]")
        "s_waitcnt vmcnt(0)\n\t"
        MFMA "v[100:103], %[a00], v[232:235], 0\n\t"
        "v_max_u32 v128, v234, v235\n\tv_max3_u32 v128, v232, v233, v128\n\t"
        MFMA "v[104:107], %[a10], v[232:235], 0\n\t"
        "v_cmp_eq_u32 vcc, -1, v128\n\ts_cbranch_vccnz 0b\n3:\n\t"
        "ds_write_b128 %[mw], v[232:235]\n\t"
        "v_mov_b32_dpp v236, v232" ROR8 "v_mov_b32_dpp v237, v233" ROR8
        MFMA "v[124:127], %[a60], v[232:235], 0\n\t"
        "v_mov_b32_dpp v238, v234" ROR8 "v_mov_b32_dpp v239, v235" ROR8
        MFMA "v[108:111], %[a20], v[232:235], 0\n\t"
        MFMA "v[112:115], %[a30], v[232:235], 0\n\t"
        MFMA "v[100:103], %[a01], v[236:239], v[100:103]\n\t"
        MFMA "v[104:107], %[a11], v[236:239], v[104:107]\n\t"
        MFMA "v[124:127], %[a61], v[236:239], v[124:127]\n\t"
        MFMA "v[108:111], %[a21], v[236:239], v[108:111]\n\t"
        MFMA "v[112:115], %[a31], v[236:239], v[112:115]\n\t"
        MFMA "v[116:119], %[a40], v[232:235], 0\n\t"
        MFMA "v[120:123], %[a50], v[232:235], 0\n\t"
        "s_waitcnt lgkmcnt(0)\n\ts_barrier\n\t"
        "ds_read_b128 v[128:131], %[mb]\n\tds_read_b64 v[156:157], %[mh]\n\tds_read_b128 v[132:135], %[mb] offset:512\n\tds_read_b128 v[136:139], %[mb] offset:1024\n\t"
        MFMA "v[116:119], %[a41], v[236:239], v[116:119]\n\t"
        CVT "v140, v100, v101\n\t" CVT "v141, v102, v103\n\t"
        MFMA "v[120:123], %[a51], v[236:239], v[120:123]\n\t"
        CVT "v142, v104, v105\n\t" CVT "v143, v106, v107\n\t"
        CVT "v152, v124, v125\n\t" CVT "v153, v126, v127\n\t"
        CVT "v144, v108, v109\n\t" CVT "v145, v110, v111\n\t"
        "s_waitcnt lgkmcnt(3)\n\t"
        MFMA "v[100:103], v[140:143], v[128:131], 0\n\t"
        CVT "v146, v112, v113\n\t" CVT "v147, v114, v115\n\t"
        "s_waitcnt lgkmcnt(2)\n\t"
        MFMA "v[100:103], v[152:155], v[156:159], v[100:103]\n\t"
        CVT "v148, v116, v117\n\t" CVT "v149, v118, v119\n\t"
        "s_waitcnt lgkmcnt(1)\n\t"
        MFMA "v[100:103], v[144:147], v[132:135], v[100:103]\n\t"
        CVT "v150, v120, v121\n\t" CVT "v151, v122, v123\n\t"
        "s_waitcnt lgkmcnt(0)\n\ts_nop 0\n\t"
        MFMA "v[100:103], v[148:151], v[136:139], v[100:103]\n\t"
        "s_nop 7\n\t"
        "ds_write_b128 %[red], v[100:103]\n\ts_waitcnt lgkmcnt(0)\n\ts_barrier"
        : [bud] "+s"(bud), "=&{v[140:143]}"(k0), "=&{v[144:147]}"(k1), "=&{v[148:151]}"(k2), "+{v[152:155]}"(kh), "+{v[156:159]}"(bh)
        : [mo] "v"(mo), [R] "s"(R), [so] "s"(ho_blk(s, 0, pol0, pol1)), [mw] "v"(lb + L_M + mo), [mb] "v"(mb), [mh] "v"(mh), [red] "v"(red),
          [a00] "v"(A[0][0]), [a01] "v"(A[0][1]), [a10] "v"(A[1][0]), [a11] "v"(A[1][1]), [a20] "v"(A[2][0]), [a21] "v"(A[2][1]), [a30] "v"(A[3][0]), [a31] "v"(A[3][1]),
          [a40] "v"(A[4][0]), [a41] "v"(A[4][1]), [a50] "v"(A[5][0]), [a51] "v"(A[5][1]), [a60] "v"(A[6][0]), [a61] "v"(A[6][1])
        : "memory", "vcc", "scc", "v100", "v101", "v102", "v103", "v104", "v105", "v106", "v107", "v108", "v109", "v110", "v111", "v112", "v113", "v114", "v115",
          "v116", "v117", "v118", "v119", "v120", "v121", "v122", "v123", "v124", "v125", "v126", "v127", "v128", "v129", "v130", "v131", "v132", "v133", "v134", "v135",
          "v136", "v137", "v138", "v139", "v232", "v233", "v234", "v235", "v236", "v237", "v238", "v239");
      ST_AT(2);
      const unsigned xb = L_X + (unsigned)(s & 3) * 8192u;
#pragma unroll
      for (int tt = 0; tt < 2; ++tt) {
        f32x4 sx = mma(k0, lds16(L, xb + tt * 4096 + lane * 16), (f32x4){0.f, 0.f, 0.f, 0.f});
        sx = mma(k1, lds16(L, xb + tt * 4096 + 1024 + lane * 16), sx);
        sx = mma(k2, lds16(L, xb + tt * 4096 + 2048 + lane * 16), sx);
        const v2u xh = lds8(L, xb + tt * 4096 + 3072 + lane * 16 + 8 * (p & 1));
        sx = mma(kh, (v4u){xh.x, xh.y, 0u, 0u}, sx);
        if (g < 2) *(LAS f32x4*)(L + L_RED2 + w * 1024 + (tt * 32 + rho * 2 + g) * 16) = sx;
      }
      WG_BAR();
      ST_AT(3);
      ST_END(s);
    }
  } else {
    auto fetch = [&](int n) {
      const int sg = n < NSEG ? n : NSEG - 1;
#pragma unroll
      for (int tt = 0; tt < 2; ++tt) {
        dma<3, false>(R, lb + L_X + (n & 3) * 8192 + tt * 4096, OFF_XF + (unsigned)((sg * 2 + tt) * 14 + 3 * p) * 1024u, lane * 16);
        dma<1, false>(R, lb + L_X + (n & 3) * 8192 + tt * 4096 + 3072, OFF_XF + (unsigned)((sg * 2 + tt) * 14 + 12 + (p >> 1)) * 1024u, lane * 16);
      } };
    fetch(0); fetch(1); fetch(2);
    WAIT_VM(0);
    __builtin_amdgcn_s_setprio(2);
    for (int s = 0; s < NSEG; ++s) {
      const unsigned so = ho_blk(s, 1, pol0, pol1) + (unsigned)((h >> 1) * 1024 + p * 256 + (h & 1) * 128);
      WG_BAR();
      ST_AT(2);
      WG_BAR();
      { f32x2 t[7];
#pragma unroll
        for (int i = 0; i < 7; ++i) t[i] = lds8f(L, L_RED + i * 256 + (lane & 31) * 8);
        const f32x2 u = ((t[0] + t[1]) + (t[2] + t[3])) + ((t[4] + t[5]) + t[6]);
        st4c(R, so, (lane & 31) * 4, fixs(pack2(u[0], u[1])));
        ST_AT(0); }
      WG_BAR();
      { f32x4 t[7];
#pragma unroll
        for (int i = 0; i < 7; ++i) t[i] = lds16f(L, L_RED2 + i * 1024 + lane * 16);
        const f32x4 u = ((t[0] + t[1]) + (t[2] + t[3])) + ((t[4] + t[5]) + t[6]);
        st8c(R, OFF_SXM + (unsigned)((s * 16 + h) * 8 + p) * 256u, (lane >> 5) * 1024 + (lane & 31) * 8, (v2u){fixs(pack2(u[0], u[1])), fixs(pack2(u[2], u[3]))}); }
      fetch(s + 3);
      WAIT_VM(20);
      ST_AT(1);
      ST_END(s);
    }
  }
  ST_DUMP();
}

DEV void role_x(rsrc_t R, LAS char* L, int h, int w, int lane, unsigned pol0, unsigned pol1) {
  const int g = lane >> 4, rho = lane & 15, hd = rho >> 3, r = rho & 7;
  constexpr unsigned L_K = 7168;
  const unsigned lb = (unsigned)(size_t)L;
  ST_DECL;
  if (w < 7) {
    int bud = POLL_BUDGET;
    const unsigned mo = (unsigned)(((2 * w + hd) * 4 + g) * 128 + r * 16);
    for (int s = 0; s < NSEG - 1; ++s) {
      WG_BAR();
      v4u kx[2][2];
#pragma unroll
      for (int i = 0; i < 4; ++i) kx[i >> 1][i & 1] = lds16(L, L_K + (s & 3) * 28672 + ((i >> 1) * 14 + 2 * w + (i & 1)) * 1024 + lane * 16);
      const v4u f0 = poll16(R, ho_blk(s, 0, pol0, pol1), mo, bud);
      ST_AT(0);
      const v4u f1 = ror8v(f0);
      f32x4 a0 = mma(kx[0][0], f0, (f32x4){0.f, 0.f, 0.f, 0.f}), a1 = mma(kx[1][1], f0, (f32x4){0.f, 0.f, 0.f, 0.f});
      a0 = mma(kx[0][1], f1, a0); a1 = mma(kx[1][0], f1, a1);
      f32x4 o;
#pragma unroll
      for (int i = 0; i < 4; ++i) o[i] = hd ? a1[i] : a0[i];
      *(LAS f32x4*)(L + w * 1024 + (r * 8 + ((g * 2 + hd) ^ r)) * 16) = o;
      WG_BAR();
      ST_AT(2);
      ST_END(s);
    }
  } else {
    unsigned cap = SPIN_CAP;
    Fetch F = {0u, 0u, 0u, 0ull};
    auto fetch = [&](unsigned must, unsigned lim) {
      for (int i = 0; i < 2 && F.go(R, lane, must, lim, i == 0, cap); ++i) {
#pragma unroll
        for (int c = 0; c < 7; ++c) dma<4, true>(R, lb + L_K + (F.nxt & 3u) * 28672u + c * 4096, OFF_KX + ((F.nxt * 16u + h) * 28u + 4 * c) * 1024u, lane * 16);
        F.issued(28);
      } };
    fetch(0, 1);
    const int kg = (lane & 7) ^ (lane >> 3);
    const unsigned eo = (unsigned)(((kg >> 1) * 8 + (lane >> 3)) * 16 + (kg & 1) * 8), lo = (unsigned)(lane >> 3) * 4u;
    for (int s = 0; s < NSEG - 1; ++s) {
      const unsigned n = F.since(s);
      if (n >= 60u) WAIT_VM(60); else if (n >= 28u) WAIT_VM(28); else WAIT_VM(0);
      WG_BAR();
      ST_AT(1);
#define DPPX(op, d, ctl) "s_nop 1\n\t" op "_dpp " d ", " d ", " d " " ctl " row_mask:0xf bank_mask:0xf\n\t"
      asm volatile("s_waitcnt lgkmcnt(0)\n\ts_barrier\n\t"
        "ds_read_b128 v[100:103], %[la]\n\tds_read_b128 v[104:107], %[la] offset:1024\n\tds_read_b128 v[108:111], %[la] offset:2048\n\tds_read_b128 v[112:115], %[la] offset:3072\n\t"
        "ds_read_b128 v[116:119], %[la] offset:4096\n\tds_read_b128 v[120:123], %[la] offset:5120\n\tds_read_b128 v[124:127], %[la] offset:6144\n\t"
        "s_waitcnt lgkmcnt(5)\n\tv_pk_add_f32 v[100:101], v[100:101], v[104:105]\n\tv_pk_add_f32 v[102:103], v[102:103], v[106:107]\n\t"
        "s_waitcnt lgkmcnt(3)\n\tv_pk_add_f32 v[108:109], v[108:109], v[112:113]\n\tv_pk_add_f32 v[110:111], v[110:111], v[114:115]\n\t"
        "s_waitcnt lgkmcnt(1)\n\tv_pk_add_f32 v[116:117], v[116:117], v[120:121]\n\tv_pk_add_f32 v[118:119], v[118:119], v[122:123]\n\t"
        "v_pk_add_f32 v[100:101], v[100:101], v[108:109]\n\tv_pk_add_f32 v[102:103], v[102:103], v[110:111]\n\t"
        "s_waitcnt lgkmcnt(0)\n\tv_pk_add_f32 v[116:117], v[116:117], v[124:125]\n\tv_pk_add_f32 v[118:119], v[118:119], v[126:127]\n\t"
        "v_pk_add_f32 v[100:101], v[100:101], v[116:117]\n\tv_pk_add_f32 v[102:103], v[102:103], v[118:119]\n\t"
        "v_max_f32 v104, v100, v101\n\tv_max3_f32 v104, v102, v103, v104\n\t"
        DPPX("v_max_f32", "v104", "quad_perm:[1,0,3,2]") DPPX("v_max_f32", "v104", "quad_perm:[2,3,0,1]") DPPX("v_max_f32", "v104", "row_half_mirror")
        "v_sub_f32 v100, v100, v104\n\tv_sub_f32 v101, v101, v104\n\tv_sub_f32 v102, v102, v104\n\tv_sub_f32 v103, v103, v104\n\t"
        "v_exp_f32 v100, v100\n\tv_exp_f32 v101, v101\n\tv_exp_f32 v102, v102\n\tv_exp_f32 v103, v103\n\t"
        "v_add_f32 v105, v100, v101\n\tv_add_f32 v106, v102, v103\n\tv_add_f32 v105, v105, v106\n\t"
        DPPX("v_add_f32", "v105", "quad_perm:[1,0,3,2]") DPPX("v_add_f32", "v105", "quad_perm:[2,3,0,1]") DPPX("v_add_f32", "v105", "row_half_mirror")
        "v_rcp_f32 v106, v105\n\tv_log_f32 v105, v105\n\t"
        "v_mul_f32 v100, v100, v106\n\tv_mul_f32 v101, v101, v106\n\tv_mul_f32 v102, v102, v106\n\tv_mul_f32 v103, v103, v106\n\t"
        CVT "v100, v100, v101\n\t" CVT "v101, v102, v103\n\t"
        "v_add_f32 v104, v104, v105\n\t"
        "v_min_u32 v100, -2, v100\n\tv_min_u32 v101, -2, v101\n\tv_min_u32 v104, -2, v104\n\t"
        "buffer_store_dwordx2 v[100:101], %[eo], %[R], %[se] offen sc1\n\tbuffer_store_dword v104, %[lo], %[R], %[sl] offen sc1"
        :: [la] "v"(lb + lane * 16), [eo] "v"(eo), [lo] "v"(lo), [R] "s"(R), [se] "s"(ho_blk(s, 2, pol0, pol1) + (unsigned)h * 512u), [sl] "s"(ho_blk(s, 0, pol0, pol1) + 7168u + (unsigned)h * 64u)
        : "memory", "v100", "v101", "v102", "v103", "v104", "v105", "v106", "v107", "v108", "v109", "v110", "v111", "v112", "v113", "v114", "v115",
          "v116", "v117", "v118", "v119", "v120", "v121", "v122", "v123", "v124", "v125", "v126", "v127");
      ST_AT(0);
      F.T += 2;
      fetch(s + 1, s + 3 < NSEG - 2 ? s + 3 : NSEG - 2);
      ST_END(s);
    }
  }
  ST_DUMP();
}

template <int TYPE>
DEV void role_g(rsrc_t R, char* wsb, LAS char* L, int gi, int w, int lane) {
  const int wc = w & 3, ws = w >> 2, g = lane >> 4, rho = lane & 15, t0 = 8 * gi + 2 * wc;
  const unsigned lb = (unsigned)(size_t)L;
  dma<4, false>(R, lb + w * 7168, OFF_XF + (unsigned)w * 7168u, lane * 16); dma<3, false>(R, lb + w * 7168 + 4096, OFF_XF + (unsigned)w * 7168u + 4096u, lane * 16);
  v4u W[2][14];
#pragma unroll
  for (int c = 0; c < 2; ++c)
#pragma unroll
    for (int k = 0; k < 14; ++k) W[c][k] = ld16p(R, OFF_WF + (unsigned)((t0 + c) * 14 + k) * 1024u, lane * 16);
  __builtin_amdgcn_s_waitcnt(0x0F70);
  WG_BAR();
  const unsigned ctl = TYPE == 2 ? CTL_H : CTL_KV;
  for (int it = 0; it < 32; ++it) {
    LAS char* cur = L + (it & 1) * 57344 + ws * 28672 + lane * 16;
    if (it + 1 < 32) {
      const unsigned dst = lb + ((it + 1) & 1) * 57344 + w * 7168, src = OFF_XF + (unsigned)((it + 1) * 56 + w * 7) * 1024u;
      dma<4, false>(R, dst, src, lane * 16); dma<3, false>(R, dst + 4096, src + 4096u, lane * 16);
    }
    f32x4 acc[2][2];
#pragma unroll
    for (int c = 0; c < 2; ++c) acc[c][0] = acc[c][1] = (f32x4){0.f, 0.f, 0.f, 0.f};
#pragma unroll
    for (int k = 0; k < 14; ++k) {
      const v4u x0 = *(LAS v4u*)(cur + k * 1024), x1 = *(LAS v4u*)(cur + 14336 + k * 1024);
#pragma unroll
      for (int c = 0; c < 2; ++c) {
        if (TYPE == 1) { acc[c][0] = mma(x0, W[c][k], acc[c][0]); acc[c][1] = mma(x1, W[c][k], acc[c][1]); }
        else { acc[c][0] = mma(W[c][k], x0, acc[c][0]); acc[c][1] = mma(W[c][k], x1, acc[c][1]); }
      }
    }
    const int s = 2 * it + ws;
    if (TYPE == 0) {
      const int h = t0 / 28, u = (t0 % 28) >> 1;
#pragma unroll
      for (int tt = 0; tt < 2; ++tt) st16c(R, OFF_KX + (unsigned)(((s * 16 + h) * 2 + tt) * 14 + u) * 1024u, lane * 16, pack8(acc[0][tt], acc[1][tt]));
    } else if (TYPE == 1) {
#pragma unroll
      for (int c = 0; c < 2; ++c) st16c(R, OFF_DX + (unsigned)(s * 448 + (t0 - 448) + c) * 1024u, lane * 16, pack8(acc[c][0], acc[c][1]));
    } else {
      const int F = (t0 - 896) >> 1;
      const f32x4 ha = acc[0][0] * acc[1][0], hb = acc[0][1] * acc[1][1];
      const auto r0 = __builtin_amdgcn_permlane16_swap(pack2(ha[0], ha[1]), pack2(hb[0], hb[1]), false, false);
      const auto r1 = __builtin_amdgcn_permlane16_swap(pack2(ha[2], ha[3]), pack2(hb[2], hb[3]), false, false);
      st16c(R, OFF_HF + (unsigned)(s * 112 + (F >> 1)) * 1024u + (unsigned)(F & 1) * 512u, (g & 1) * 57344 + ((g >> 1) * 16 + rho) * 16, (v4u){r0[0], r1[0], r0[1], r1[1]});
    }
    if (it == 0 || it == 31) WAIT_VM(0); else if (TYPE == 2) WAIT_VM(1); else WAIT_VM(2);
    WG_BAR();
    if (w == 0 && lane == 0) {
      if (it == 0) signal(wsb, ctl);
      else if (it > 1) signal(wsb, ctl + (it - 1) * 256);
      if (it == 31) signal(wsb, ctl + 31 * 256);
    }
  }
}

DEV void role_f1(rsrc_t R, char* wsb, LAS char* L, int task, int w, int lane, unsigned& cap) {
  const int s = task >> 1, h = (task & 1) * 8 + w, g = lane >> 4, rho = lane & 15;
  if (w < 7) dma<4, false>(R, (unsigned)(size_t)L + w * 4096, OFF_XF + (unsigned)(s * 28 + w * 4) * 1024u, lane * 16);
  unsigned cnt = 0;
  wait_cnt(R, CTL_KV, lane, 32, cnt, s >> 1, 112, cap, true);
  v4u kx[28];
#pragma unroll
  for (int i = 0; i < 28; ++i) kx[i] = ld16c(R, OFF_KX + (unsigned)((s * 16 + h) * 28 + i) * 1024u, lane * 16);
  WAIT_VM(0);
  WG_BAR();
  f32x4 a00 = {0.f, 0.f, 0.f, 0.f}, a01 = a00, a11 = a00;
#pragma unroll
  for (int k = 0; k < 14; ++k) {
    const v4u x0 = lds16(L, k * 1024 + lane * 16), x1 = lds16(L, 14336 + k * 1024 + lane * 16);
    a00 = mma(kx[k], x0, a00); a01 = mma(kx[k], x1, a01); a11 = mma(kx[14 + k], x1, a11);
  }
  const float NEG = -3.0e38f;
#pragma unroll
  for (int i = 0; i < 4; ++i) { const bool ok = 4 * g + i <= rho; a00[i] = ok ? a00[i] : NEG; a11[i] = ok ? a11[i] : NEG; }
  const float m0 = x32max(x16max(fmaxf(fmaxf(a00[0], a00[1]), fmaxf(a00[2], a00[3]))));
  const float m1 = x32max(x16max(fmaxf(fmaxf(fmaxf(a01[0], a01[1]), fmaxf(a01[2], a01[3])), fmaxf(fmaxf(a11[0], a11[1]), fmaxf(a11[2], a11[3])))));
#pragma unroll
  for (int i = 0; i < 4; ++i) { a00[i] = ex2(a00[i] - m0); a01[i] = ex2(a01[i] - m1); a11[i] = ex2(a11[i] - m1); }
  const float l0 = x32add(x16add((a00[0] + a00[1]) + (a00[2] + a00[3])));
  const float l1 = x32add(x16add(((a01[0] + a01[1]) + (a01[2] + a01[3])) + ((a11[0] + a11[1]) + (a11[2] + a11[3]))));
  const float i0 = rcp(l0), i1 = rcp(l1);
  st16c(R, OFF_EX + (unsigned)((s * 16 + h) * 2) * 1024u, lane * 16, pack8(a00 * i0, (f32x4){0.f, 0.f, 0.f, 0.f}));
  st16c(R, OFF_EX + (unsigned)((s * 16 + h) * 2 + 1) * 1024u, lane * 16, pack8(a01 * i1, a11 * i1));
  if (g < 2) st4c(R, OFF_LX + (unsigned)(s * 16 + h) * 128u, lane * 4, __builtin_bit_cast(unsigned, g ? m1 + lg2(l1) : m0 + lg2(l0)));
  WAIT_VM(0);
  WG_BAR();
  if (w == 0 && lane == 0) signal(wsb, CTL_E + s * 256);
}

DEV v4u scale8(v4u e, float a) {
  v4u r;
#pragma unroll
  for (int q = 0; q < 4; ++q) r[q] = pack2(bflo(e[q]) * a, bfhi(e[q]) * a);
  return r;
}

DEV void role_f2(rsrc_t R, const MegaArgs& a, LAS char* L, int q, int grp, int w, int lane, unsigned& cap) {
  const int g = lane >> 4, rho = lane & 15;
  v4u Wd[7][4];
#pragma unroll
  for (int k = 0; k < 7; ++k)
#pragma unroll
    for (int c = 0; c < 4; ++c) Wd[k][c] = ld16p(R, OFF_WD + (unsigned)((4 * q + c) * 56 + 7 * w + k) * 1024u, lane * 16);
  const v4u sel0 = onehot(0, rho < 8 ? rho : -1), sel1 = onehot(0, rho >= 8 ? rho - 8 : -1);
  for (int s = grp; s < NSEG; s += 21) {
    unsigned cnt = 0;
    wait_cnt(R, CTL_H, lane, 32, cnt, s >> 1, 28, cap, true);
    f32x4 acc[4][2];
#pragma unroll
    for (int c = 0; c < 4; ++c) acc[c][0] = acc[c][1] = (f32x4){0.f, 0.f, 0.f, 0.f};
    { v4u hf[14];
#pragma unroll
      for (int i = 0; i < 14; ++i) hf[i] = ld16c(R, OFF_HF + (unsigned)((s * 2 + i / 7) * 56 + 7 * w + i % 7) * 1024u, lane * 16);
#pragma unroll
      for (int k = 0; k < 7; ++k)
#pragma unroll
        for (int c = 0; c < 4; ++c) { acc[c][0] = mma(Wd[k][c], hf[k], acc[c][0]); acc[c][1] = mma(Wd[k][c], hf[7 + k], acc[c][1]); } }
    cnt = 0;
    wait_cnt(R, CTL_E, lane, 64, cnt, s, 2, cap, true);
    cnt = 0;
    wait_cnt(R, CTL_KV, lane, 32, cnt, s >> 1, 112, cap, true);
    v4u D[2][4], E[2][2]; float lse[2];
#pragma unroll
    for (int hh = 0; hh < 2; ++hh) {
      const unsigned sh = (unsigned)(s * 16 + 2 * w + hh);
#pragma unroll
      for (int c = 0; c < 4; ++c) D[hh][c] = ld16c(R, OFF_DX + (sh * 28 + 4 * q + c) * 1024u, lane * 16);
      E[hh][0] = ld16c(R, OFF_EX + sh * 2048u, lane * 16); E[hh][1] = ld16c(R, OFF_EX + sh * 2048u + 1024u, lane * 16);
      lse[hh] = __builtin_bit_cast(float, ld4c(R, OFF_LX + sh * 128u, ((g >> 1) * 16 + rho) * 4));
    }
    v4u sx[4], mp[4];
    for (unsigned n = 0;; ++n) {
      unsigned m = 0;
#pragma unroll
      for (int i = 0; i < 4; ++i) {
        sx[i] = ld16c(R, OFF_SXM + (unsigned)((s * 16 + 2 * w) * 2 + i) * 1024u, lane * 16);
        mp[i] = ld16c(R, OFF_MP + (unsigned)((s * 8 + w) * 28 + 4 * q + i) * 512u, (lane & 31) * 16);
      }
#pragma unroll
      for (int i = 0; i < 4; ++i) m = umax(m, umax(max4u(sx[i]), max4u(mp[i])));
      if (__all(m != SENT)) break;
      if (n >= cap) { cap = 0; break; }
      __builtin_amdgcn_s_sleep(16);
      CFENCE();
    }
    v4u bm; float al[2][2];
#pragma unroll
    for (int hh = 0; hh < 2; ++hh) {
      f32x4 sv = mma(sel0, sx[2 * hh], (f32x4){0.f, 0.f, 0.f, 0.f});
      sv = mma(sel1, sx[2 * hh + 1], sv);
      const float mx = fmaxf(x16max(fmaxf(fmaxf(sv[0], sv[1]), fmaxf(sv[2], sv[3]))), lse[hh]);
      const float e0 = ex2(sv[0] - mx), e1 = ex2(sv[1] - mx), e2 = ex2(sv[2] - mx), e3 = ex2(sv[3] - mx), ea = ex2(lse[hh] - mx);
      const float inv = rcp(x16add((e0 + e1) + (e2 + e3)) + ea);
      bm[2 * hh] = pack2(e0 * inv, e1 * inv); bm[2 * hh + 1] = pack2(e2 * inv, e3 * inv);
      const unsigned au = __builtin_bit_cast(unsigned, ea * inv);
      const auto rr = __builtin_amdgcn_permlane32_swap(au, au, false, false);
      al[hh][0] = __builtin_bit_cast(float, (unsigned)rr[0]); al[hh][1] = __builtin_bit_cast(float, (unsigned)rr[1]);
    }
    const v4u zz = {0u, 0u, 0u, 0u};
    const v4u b0 = g < 2 ? bm : zz, b1 = g < 2 ? zz : bm;
#pragma unroll
    for (int c = 0; c < 4; ++c) { acc[c][0] = mma(mp[c], b0, acc[c][0]); acc[c][1] = mma(mp[c], b1, acc[c][1]); }
#pragma unroll
    for (int hh = 0; hh < 2; ++hh)
#pragma unroll
      for (int tt = 0; tt < 2; ++tt) {
        const v4u es = scale8(E[hh][tt], al[hh][tt]);
#pragma unroll
        for (int c = 0; c < 4; ++c) acc[c][tt] = mma(D[hh][c], es, acc[c][tt]);
      }
#pragma unroll
    for (int tt = 0; tt < 2; ++tt)
#pragma unroll
      for (int c = 0; c < 4; ++c) *(LAS f32x4*)(L + w * 8192 + (tt * 4 + c) * 1024 + lane * 16) = acc[c][tt];
    const size_t oi = (size_t)(s * 32 + (w >> 2) * 16 + rho) * DM + 64 * q + 16 * (w & 3) + 4 * g;
    const v2u xv = *(const GAS v2u*)(a.x + oi);
    WG_BAR();
    f32x4 t[8];
#pragma unroll
    for (int i = 0; i < 8; ++i) t[i] = lds16f(L, i * 8192 + w * 1024 + lane * 16);
    const f32x4 u = ((t[0] + t[1]) + (t[2] + t[3])) + ((t[4] + t[5]) + (t[6] + t[7]));
    *(GAS v2u*)(a.out + oi) = (v2u){pack2(u[0] + bflo(xv.x), u[1] + bfhi(xv.x)), pack2(u[2] + bflo(xv.y), u[3] + bfhi(xv.y))};
    WG_BAR();
  }
}

__global__ void __launch_bounds__(512, 2) mega_kernel(MegaArgs a) {
  extern __shared__ __attribute__((aligned(16))) char smem[];
  LAS char* L = (LAS char*)smem;
  const int b = blockIdx.x, w = __builtin_amdgcn_readfirstlane(threadIdx.x >> 6), lane = threadIdx.x & 63;
  char* const wsb = ws_base(a.ws);
  const rsrc_t R = __builtin_amdgcn_make_buffer_rsrc(wsb, 0, WS_USED, 0x00020000);
  const int side = (b >> 2) & 1, idx = (b >> 3) * 4 + (b & 3);
  if (side == 0 && idx < 108) {
    const unsigned pol0 = __builtin_amdgcn_readfirstlane(__builtin_amdgcn_raw_buffer_load_b32(R, CTL_POL, 0, 0)) & 8192u;
    const unsigned pol1 = __builtin_amdgcn_readfirstlane(__builtin_amdgcn_raw_buffer_load_b32(R, CTL_POL + 4, 0, 0)) & 8192u;
    if (idx < 28) role_v(R, L, idx, w, lane, pol0, pol1);
    else if (idx < 44) role_x(R, L, idx - 28, w, lane, pol0, pol1);
    else role_q(R, L, (idx - 44) >> 2, (idx - 44) & 3, w, lane, pol0, pol1);
    return;
  }
  const int bi = side ? idx : 128 + idx - 108;
  ST_DECL;
  ST_AT(0);
  if (bi < 56) role_g<0>(R, wsb, L, bi, w, lane);
  else if (bi < 112) role_g<1>(R, wsb, L, bi, w, lane);
  else if (bi < 140) role_g<2>(R, wsb, L, bi, w, lane);
  ST_AT(1);
  unsigned cap = SPIN_CAP;
  if (bi < 128) role_f1(R, wsb, L, bi, w, lane, cap);
  ST_AT(2);
  if (bi < 147) role_f2(R, a, L, bi % 7, bi / 7, w, lane, cap);
  ST_AT(3);
  ST_END(0);
  ST_DUMP();
}

extern "C" void kernel_launch(void* const* d_in, const int* in_sizes, int n_in, void* d_out, int out_size, void* d_ws, size_t ws_size, hipStream_t stream) {
  ProArgs p;
  p.x = (const unsigned short*)d_in[0]; p.mem0 = (const unsigned short*)d_in[1];
  p.wq = (const unsigned short*)d_in[2]; p.wk = (const unsigned short*)d_in[3]; p.wv = (const unsigned short*)d_in[4]; p.wo = (const unsigned short*)d_in[5];
  p.wg = (const unsigned short*)d_in[8]; p.wu = (const unsigned short*)d_in[9]; p.wd = (const unsigned short*)d_in[10];
  p.ws = (char*)d_ws;
  pro_kernel<<<PRO_GRID, 512, 0, stream>>>(p);
  MegaArgs m; m.x = p.x; m.out = (unsigned short*)d_out; m.ws = (char*)d_ws;
  (void)hipFuncSetAttribute((const void*)mega_kernel, hipFuncAttributeMaxDynamicSharedMemorySize, LDS_BYTES);
  mega_kernel<<<256, 512, LDS_BYTES, stream>>>(m);
}
```

```cpp
#include <hip/hip_runtime.h>
#include <hip/hip_bf16.h>
#include <type_traits>

typedef unsigned v4u __attribute__((ext_vector_type(4)));
typedef unsigned v2u __attribute__((ext_vector_type(2)));
typedef float f32x4 __attribute__((ext_vector_type(4)));
typedef float f32x2 __attribute__((ext_vector_type(2)));
typedef __bf16 bf16x8 __attribute__((ext_vector_type(8)));
typedef __bf16 bf16x2 __attribute__((ext_vector_type(2)));
typedef __amdgpu_buffer_rsrc_t rsrc_t;
#define LAS __attribute__((address_space(3)))
#define GAS __attribute__((address_space(1)))
#define DEV __device__ __forceinline__

constexpr int S_ = 2048, DM = 448, NH = 16, DH = 256, FF = 1792, NSEG = 64;
constexpr unsigned MB = 1u << 20;
constexpr unsigned OFF_HO = 0;
constexpr unsigned OFF_CTL = 4 * MB;
constexpr unsigned CTL_KV = OFF_CTL;
constexpr unsigned CTL_H = OFF_CTL + 8192;
constexpr unsigned CTL_E = OFF_CTL + 16384;
constexpr unsigned CTL_P = OFF_CTL + 32768;
constexpr unsigned CTL_POL = OFF_CTL + 49152;
constexpr unsigned CTL_BYTES = 49152;
constexpr unsigned OFF_DBG = 101 * MB;
constexpr unsigned OFF_SXM = 5 * MB;
constexpr unsigned OFF_MP = 7 * MB;
constexpr unsigned OFF_XF = 14 * MB;
constexpr unsigned OFF_EX = 18 * MB;
constexpr unsigned OFF_LX = 20 * MB;
constexpr unsigned OFF_HF = 21 * MB;
constexpr unsigned OFF_WF = 28 * MB;
constexpr unsigned OFF_KX = 44 * MB;
constexpr unsigned OFF_DX = 72 * MB;
constexpr unsigned WS_USED = 104 * MB;
constexpr unsigned SENT = 0xFFFFFFFFu;
constexpr unsigned SPIN_CAP = 1u << 19;
constexpr int POLL_BUDGET = 1 << 20;
constexpr int LDS_BYTES = 131072;

DEV unsigned pack2(float lo, float hi) { f32x2 f = {lo, hi}; return __builtin_bit_cast(unsigned, __builtin_convertvector(f, bf16x2)); }
DEV v4u pack8(f32x4 a, f32x4 b) { return (v4u){pack2(a[0], a[1]), pack2(a[2], a[3]), pack2(b[0], b[1]), pack2(b[2], b[3])}; }
DEV unsigned fixs(unsigned x) { return x < 0xFFFFFFFEu ? x : 0xFFFFFFFEu; }
DEV v4u fixs4(v4u v) { return (v4u){fixs(v.x), fixs(v.y), fixs(v.z), fixs(v.w)}; }
DEV float bflo(unsigned u) { return __builtin_bit_cast(float, u << 16); }
DEV float bfhi(unsigned u) { return __builtin_bit_cast(float, u & 0xFFFF0000u); }
DEV f32x4 mma(v4u a, v4u b, f32x4 c) { return __builtin_amdgcn_mfma_f32_16x16x32_bf16(__builtin_bit_cast(bf16x8, a), __builtin_bit_cast(bf16x8, b), c, 0, 0, 0); }
DEV v4u ld16c(rsrc_t r, unsigned so, unsigned vo) { return __builtin_amdgcn_raw_buffer_load_b128(r, vo, so, 16); }
DEV unsigned ld4c(rsrc_t r, unsigned so, unsigned vo) { return __builtin_amdgcn_raw_buffer_load_b32(r, vo, so, 16); }
DEV v4u ld16p(rsrc_t r, unsigned so, unsigned vo) { return __builtin_amdgcn_raw_buffer_load_b128(r, vo, so, 0); }
DEV void st16c(rsrc_t r, unsigned so, unsigned vo, v4u v) { __builtin_amdgcn_raw_buffer_store_b128(v, r, vo + so, 0, 16); }
DEV void st8c(rsrc_t r, unsigned so, unsigned vo, v2u v) { __builtin_amdgcn_raw_buffer_store_b64(v, r, vo, so, 16); }
DEV void st4c(rsrc_t r, unsigned so, unsigned vo, unsigned v) { __builtin_amdgcn_raw_buffer_store_b32(v, r, vo, so, 16); }
DEV void st16p(rsrc_t r, unsigned so, unsigned vo, v4u v) { __builtin_amdgcn_raw_buffer_store_b128(v, r, vo + so, 0, 0); }
DEV unsigned umax(unsigned a, unsigned b) { return a > b ? a : b; }
DEV unsigned max4u(v4u v) { return umax(umax(v.x, v.y), umax(v.z, v.w)); }
DEV unsigned ror8(unsigned x) { return __builtin_amdgcn_mov_dpp(x, 0x128, 0xf, 0xf, false); }
DEV float ror8f(float x) { return __builtin_bit_cast(float, ror8(__builtin_bit_cast(unsigned, x))); }
DEV v4u ror8v(v4u v) { return (v4u){ror8(v.x), ror8(v.y), ror8(v.z), ror8(v.w)}; }
DEV float x16max(float x) { unsigned u = __builtin_bit_cast(unsigned, x); auto r = __builtin_amdgcn_permlane16_swap(u, u, false, false); return fmaxf(__builtin_bit_cast(float, (unsigned)r[0]), __builtin_bit_cast(float, (unsigned)r[1])); }
DEV float x32max(float x) { unsigned u = __builtin_bit_cast(unsigned, x); auto r = __builtin_amdgcn_permlane32_swap(u, u, false, false); return fmaxf(__builtin_bit_cast(float, (unsigned)r[0]), __builtin_bit_cast(float, (unsigned)r[1])); }
DEV float x16add(float x) { unsigned u = __builtin_bit_cast(unsigned, x); auto r = __builtin_amdgcn_permlane16_swap(u, u, false, false); return __builtin_bit_cast(float, (unsigned)r[0]) + __builtin_bit_cast(float, (unsigned)r[1]); }
DEV float x32add(float x) { unsigned u = __builtin_bit_cast(unsigned, x); auto r = __builtin_amdgcn_permlane32_swap(u, u, false, false); return __builtin_bit_cast(float, (unsigned)r[0]) + __builtin_bit_cast(float, (unsigned)r[1]); }
DEV float ex2(float x) { return __builtin_amdgcn_exp2f(x); }
DEV float lg2(float x) { return __builtin_amdgcn_logf(x); }
DEV float rcp(float x) { return __builtin_amdgcn_rcpf(x); }
#define CFENCE() asm volatile("" ::: "memory")
#define WG_BAR() do { asm volatile("s_waitcnt lgkmcnt(0)" ::: "memory"); __builtin_amdgcn_s_barrier(); asm volatile("" ::: "memory"); } while (0)
#define WAIT_VM(n) asm volatile("s_waitcnt vmcnt(" #n ")" ::: "memory")
DEV v4u lds16(LAS char* l, unsigned off) { return *(LAS v4u*)(l + off); }
DEV v2u lds8(LAS char* l, unsigned off) { return *(LAS v2u*)(l + off); }
DEV f32x4 lds16f(LAS char* l, unsigned off) { return *(LAS f32x4*)(l + off); }
DEV f32x2 lds8f(LAS char* l, unsigned off) { return *(LAS f32x2*)(l + off); }
DEV v4u onehot(int g, int want) {
  v4u r; const int i = want - 8 * g;
#pragma unroll
  for (int q = 0; q < 4; ++q) r[q] = (i == 2 * q) ? 0x3F80u : ((i == 2 * q + 1) ? 0x3F800000u : 0u);
  return r;
}
DEV unsigned ho_blk(int s, int type, unsigned pol0, unsigned pol1) { return OFF_HO + (unsigned)(s >> 5) * (2 * MB) + (unsigned)((s & 31) * 3 + type) * 16384u + ((s >> 5) ? pol1 : pol0); }

#define ST_DECL do {} while (0)
#define ST_AT(k) do {} while (0)
#define ST_END(s) do {} while (0)
#define ST_DUMP() do {} while (0)
#define PST(k) do {} while (0)

#define DMA1(o, c) "buffer_load_dwordx4 %1, %2, %3 offen" o c " lds\n\t"
#define DMA_ASM(body) asm volatile("s_mov_b32 m0, %0\n\ts_nop 3\n\t" body :: "s"(dst), "v"(vo), "s"(R), "s"(so) : "memory")
template <int N, bool COH> DEV void dma(rsrc_t R, unsigned dst, unsigned so, unsigned vo) {
  if constexpr (COH) {
    if constexpr (N == 1) DMA_ASM(DMA1("", " sc1"));
    else if constexpr (N == 3) DMA_ASM(DMA1("", " sc1") DMA1(" offset:1024", " sc1") DMA1(" offset:2048", " sc1"));
    else DMA_ASM(DMA1("", " sc1") DMA1(" offset:1024", " sc1") DMA1(" offset:2048", " sc1") DMA1(" offset:3072", " sc1"));
  } else {
    if constexpr (N == 1) DMA_ASM(DMA1("", ""));
    else if constexpr (N == 3) DMA_ASM(DMA1("", "") DMA1(" offset:1024", "") DMA1(" offset:2048", ""));
    else DMA_ASM(DMA1("", "") DMA1(" offset:1024", "") DMA1(" offset:2048", "") DMA1(" offset:3072", ""));
  }
}

#define SB() __builtin_amdgcn_sched_barrier(0)
#define POLL_HEAD "s_branch 0f\n4:\n\ts_waitcnt vmcnt(0)\n\ts_branch 3f\n0:\n\t"
#define POLL_BUD(b) "s_sub_u32 " b ", " b ", 1\n\ts_cmp_lt_i32 " b ", 0\n\ts_cbranch_scc1 4b\n\t"
DEV v4u poll16(rsrc_t R, unsigned so, unsigned vo, int& bud) {
  v4u d; unsigned t;
  asm volatile(POLL_HEAD
    "buffer_load_dwordx4 v[232:235], %3, %4, %5 offen sc1\n\t" POLL_BUD("%2")
    "s_waitcnt vmcnt(0)\n\t"
    "v_max_u32 %1, v234, v235\n\tv_max3_u32 %1, v232, v233, %1\n\tv_cmp_eq_u32 vcc, -1, %1\n\ts_cbranch_vccnz 0b\n3:"
    : "=&{v[232:235]}"(d), "=&v"(t), "+s"(bud) : "v"(vo), "s"(R), "s"(so) : "memory", "vcc", "scc");
  return d;
}
DEV void wait_cnt(rsrc_t R, unsigned base, int lane, int nlanes, unsigned& cnt, int idx, unsigned target, unsigned& cap, bool nap) {
  for (unsigned n = 0;; ++n) {
    if ((unsigned)__builtin_amdgcn_readlane(cnt, idx) >= target) break;
    if (n >= cap) { cap = 0; break; }
    if (nap) __builtin_amdgcn_s_sleep(8);
    CFENCE();
    cnt = ld4c(R, base, (unsigned)(lane & (nlanes - 1)) * 256u);
  }
  CFENCE();
}
#define TUNE_DECL constexpr int T_S = 1400, T_M = 120, T_E = 950, T_L = 0, T_X = 0; (void)T_S; (void)T_M; (void)T_E; (void)T_L; (void)T_X
DEV unsigned clk() { return (unsigned)__builtin_amdgcn_s_memtime(); }
DEV unsigned wall() { return (unsigned)__builtin_amdgcn_s_memrealtime(); }
DEV unsigned pace_target(rsrc_t R, unsigned off, int lane) {
  const unsigned x = ld4c(R, off, (unsigned)(lane < 28 ? lane : 0) * 16u), n = wall();
  int d = x == SENT ? 0 : (int)(x - n);
  d = d < 0 ? 0 : (d > 300 ? 300 : d);
  d = max(d, __builtin_amdgcn_mov_dpp(d, 0x128, 0xf, 0xf, false)); d = max(d, __builtin_amdgcn_mov_dpp(d, 0x124, 0xf, 0xf, false));
  d = max(d, __builtin_amdgcn_mov_dpp(d, 0x122, 0xf, 0xf, false)); d = max(d, __builtin_amdgcn_mov_dpp(d, 0x121, 0xf, 0xf, false));
  return n + (unsigned)max(__builtin_amdgcn_readlane(d, 0), __builtin_amdgcn_readlane(d, 16));
}
DEV void pace_wall(unsigned t) { while ((int)(wall() - t) < 0) { } CFENCE(); }
DEV void pace_clk(unsigned t) {
  int r = (int)(t - clk());
  r = r > 4096 ? 4096 : r;
  asm volatile("0:\n\ts_sub_i32 %0, %0, 32\n\ts_cmp_gt_i32 %0, 0\n\ts_cbranch_scc1 0b" : "+s"(r) :: "scc", "memory");
}
DEV void signal(char* wsb, unsigned off) { __hip_atomic_fetch_add((GAS unsigned*)(wsb + off), 1u, __ATOMIC_RELAXED, __HIP_MEMORY_SCOPE_AGENT); }

struct ProArgs { const unsigned short *x, *mem0, *wq, *wk, *wv, *wo; char* ws; };
DEV char* ws_base(char* ws) { return (char*)(((size_t)ws + 2 * MB - 1) & ~(size_t)(2 * MB - 1)); }
DEV v4u gl16(const unsigned short* p) { return *(const GAS v4u*)p; }
DEV rsrc_t rsrc_of(const void* p, unsigned bytes) { return __builtin_amdgcn_make_buffer_rsrc((void*)p, 0, bytes, 0x00020000); }
template <int CTL> DEV float rorf(float x) { return __builtin_bit_cast(float, __builtin_amdgcn_mov_dpp(__builtin_bit_cast(unsigned, x), CTL, 0xf, 0xf, false)); }
DEV float wave_sum(float x) { x += rorf<0x128>(x); x += rorf<0x124>(x); x += rorf<0x122>(x); x += rorf<0x121>(x); return x32add(x16add(x)); }

struct Tr {
  unsigned src, rd;
  DEV Tr(int lane, unsigned ld) {
    src = (unsigned)(lane >> 3) * ld + (unsigned)((lane & 7) ^ ((lane >> 3) & 2)) * 16u;
    const int g = lane >> 4, q = (lane >> 2) & 3;
    rd = (unsigned)((8 * g + q) * 128 + (lane & 3) * 8 + (((q >> 1) | ((g & 1) << 1)) << 5));
  }
  DEV void load(rsrc_t R, unsigned dst, unsigned so, unsigned ld, int t) const { dma<1, false>(R, dst + t * 1024, so + t * 8 * ld, src ^ ((t & 1) << 6)); }
  DEV v4u frag(LAS char* base, int kk, int j) const {
    typedef short v4s __attribute__((ext_vector_type(4)));
    LAS char* p = base + (rd ^ (unsigned)(j << 5)) + kk * 4096;
    const v2u lo = __builtin_bit_cast(v2u, __builtin_amdgcn_ds_read_tr16_b64_v4i16((LAS v4s*)p)), hi = __builtin_bit_cast(v2u, __builtin_amdgcn_ds_read_tr16_b64_v4i16((LAS v4s*)(p + 512)));
    return (v4u){lo.x, lo.y, hi.x, hi.y};
  }
};

DEV void role_probe(rsrc_t R, char* wsb, LAS char* L, int w, int lane) {
  unsigned best[2] = {~0u, ~0u};
  for (int i = 0; i < 6; ++i) {
    const char* p = wsb + OFF_HO + (size_t)(w & 1) * (2 * MB) + (size_t)(100 + (w >> 1 & 1)) * 16384 + (i & 1) * 8192 + (w >> 2) * 4096 + (i >> 1) * 1280;
    unsigned t;
    const unsigned long long c0 = __builtin_amdgcn_s_memtime();
    asm volatile("s_waitcnt lgkmcnt(0)\n\tglobal_load_dword %0, %1, off sc1\n\ts_waitcnt vmcnt(0)" : "=&v"(t) : "v"(p) : "memory");
    const unsigned long long c1 = __builtin_amdgcn_s_memtime();
    const unsigned d = (unsigned)(c1 - c0);
    if (i & 1) best[1] = d < best[1] ? d : best[1]; else best[0] = d < best[0] ? d : best[0];
  }
  *(LAS v2u*)(L + 131072 + w * 8) = (v2u){best[0], best[1]};
  WG_BAR();
  if (w == 0) {
    v4u m = lds16(L, 131072);
#pragma unroll
    for (int i = 1; i < 4; ++i) { const v4u t = lds16(L, 131072 + i * 16); m = (v4u){min(m.x, t.x), min(m.y, t.y), min(m.z, t.z), min(m.w, t.w)}; }
    st4c(R, CTL_POL, 0u, (m.y < m.x ? 8192u : 0u) + (m.w < m.z ? 16384u : 0u));
  }
}
constexpr int PRO_GRID = 256, PRO_LDS = 147456;
constexpr unsigned FILL_N = 3072 + 96 + 336;

__global__ void __launch_bounds__(512, 2) pro_kernel(ProArgs a) {
  extern __shared__ __attribute__((aligned(16))) char smem[];
  LAS char* L = (LAS char*)smem;
  const unsigned lb = (unsigned)(size_t)L;
  const int b = blockIdx.x, w = __builtin_amdgcn_readfirstlane(threadIdx.x >> 6), lane = threadIdx.x & 63, g = lane >> 4, rho = lane & 15;
  char* const wsb = ws_base(a.ws);
  const rsrc_t R = rsrc_of(wsb, WS_USED);
  PST(0);
  auto fill = [&]() {
    const v4u sv = {SENT, SENT, SENT, SENT};
    for (unsigned c = b + 16u; c < FILL_N; c += PRO_GRID) {
      const unsigned off = c < 1536u ? OFF_HO + c * 1024u : c < 3072u ? OFF_HO + 2 * MB + (c - 1536u) * 1024u : c < 3168u ? OFF_SXM + (61u * 32u - 3072u + c) * 1024u : OFF_MP + (61u * 112u - 3168u + c) * 1024u;
      st16p(R, off, lane * 16, sv);
    } };
  if (b < 224) {
    const int combo = (b & 7) + 8 * ((b >> 3) / 7), isv = combo >> 4, h = combo & 15, cb = (b >> 3) % 7;
    constexpr unsigned L_B = 7 * 16384;
    const unsigned rsrc_off = (unsigned)g * 8192u + (unsigned)((rho ^ g) * 16);
    const Tr T(lane, DM * 2);
    if (w == 7) {
      const rsrc_t RB = rsrc_of(isv ? a.wo : a.wq, 448u * 8192u);
#pragma unroll
      for (int t = 0; t < 32; ++t) {
        if (isv) T.load(RB, lb + L_B, (unsigned)(h * 256 * DM + 64 * cb) * 2u, DM * 2, t);
        else dma<1, false>(RB, lb + L_B + t * 1024, (unsigned)(64 * cb + 4 * (t & 15)) * 8192u + (unsigned)(h * 512 + (t >> 4) * 256), rsrc_off ^ (((t >> 1) & 3) << 6));
      }
      WAIT_VM(16); WG_BAR();
      WAIT_VM(0); WG_BAR();
      fill();
      PST(1); PST(2);
      return;
    }
    const rsrc_t RA = rsrc_of(isv ? a.wv : a.wk, 448u * 8192u);
    const unsigned sa = (unsigned)(64 * w) * 8192u + (unsigned)h * 512u;
    v4u a1[16];
#pragma unroll
    for (int t = 0; t < 16; ++t) dma<1, false>(RA, lb + w * 16384 + t * 1024, sa + t * 32768u, rsrc_off ^ (((t >> 1) & 3) << 6));
#pragma unroll
    for (int t = 0; t < 16; ++t) a1[t] = ld16p(RA, sa + t * 32768u + 256u, rsrc_off ^ (((t >> 1) & 3) << 6));
    f32x4 acc[4][4];
#pragma unroll
    for (int i = 0; i < 4; ++i)
#pragma unroll
      for (int j = 0; j < 4; ++j) acc[i][j] = (f32x4){0.f, 0.f, 0.f, 0.f};
    const unsigned rdo = (unsigned)((8 * (rho >> 2) + (rho & 3)) * 256 + ((g ^ rho) * 16));
    auto half = [&](int hf, auto trans) {
#pragma unroll
      for (int es = 0; es < 4; ++es) {
        v4u A[4], B[4];
#pragma unroll
        for (int i = 0; i < 4; ++i) {
          A[i] = lds16(L, w * 16384 + (rdo ^ (es << 6)) + (i >> 1) * 8192 + (i & 1) * 1024);
          if (trans) B[i] = T.frag(L + L_B, 4 * hf + es, i); else B[i] = lds16(L, L_B + hf * 16384 + (rdo ^ (es << 6)) + (i >> 1) * 8192 + (i & 1) * 1024);
        }
#pragma unroll
        for (int i = 0; i < 4; ++i)
#pragma unroll
          for (int j = 0; j < 4; ++j) acc[i][j] = mma(A[i], B[j], acc[i][j]);
      } };
    WAIT_VM(16); WG_BAR();
    if (isv) half(0, std::true_type()); else half(0, std::false_type());
    CFENCE();
#pragma unroll
    for (int t = 0; t < 16; ++t) *(LAS v4u*)(L + w * 16384 + t * 1024 + lane * 16) = a1[t];
    WG_BAR();
    if (isv) half(1, std::true_type()); else half(1, std::false_type());
    PST(1);
    const float sc = isv ? 1.f : 0.0625f * 1.4426950408889634f;
#pragma unroll
    for (int j = 0; j < 4; ++j)
#pragma unroll
      for (int u = 0; u < 2; ++u)
        st16p(R, OFF_WF + (unsigned)((isv * 448 + h * 28 + 4 * cb + j) * 14 + 2 * w + u) * 1024u, lane * 16, pack8(acc[2 * u][j] * sc, acc[2 * u + 1][j] * sc));
  } else {
    const int row0 = ((b - 224) * 8 + w) * 8;
    const bool act = lane < 56;
    v4u v[8];
#pragma unroll
    for (int i = 0; i < 8; ++i) v[i] = act ? gl16(a.x + (size_t)(row0 + i) * DM + lane * 8) : (v4u){0u, 0u, 0u, 0u};
    if (w == 7) fill();
    if (b == 224 && w == 5) {
      for (unsigned c = 0; c < CTL_BYTES / 1024; ++c) st16p(R, OFF_CTL + c * 1024u, lane * 16, (v4u){0u, 0u, 0u, 0u});
    } else if (b == 225 && w == 5) {
      for (int c = 0; c < 8; ++c) {
        v4u m = {SENT, SENT, SENT, SENT};
        if (c < 7) { const int pc = c * 64 + lane; m = fixs4(gl16(a.mem0 + (size_t)(pc & 7) * DM + (pc >> 3) * 8)); }
        st16p(R, OFF_HO + c * 1024u, lane * 16, m);
        st16p(R, OFF_HO + 8192u + c * 1024u, lane * 16, m);
      }
    }
#pragma unroll
    for (int i = 0; i < 8; ++i) {
      float f[8];
#pragma unroll
      for (int q = 0; q < 4; ++q) { f[2 * q] = bflo(v[i][q]); f[2 * q + 1] = bfhi(v[i][q]); }
      const float mean = wave_sum(((f[0] + f[1]) + (f[2] + f[3])) + ((f[4] + f[5]) + (f[6] + f[7]))) * (1.f / DM);
      float vs = 0.f;
#pragma unroll
      for (int q = 0; q < 8; ++q) { f[q] -= mean; vs += f[q] * f[q]; }
      const float rs = 1.f / sqrtf(wave_sum(act ? vs : 0.f) * (1.f / DM) + 1e-5f);
      const v4u o4 = {pack2(f[0] * rs, f[1] * rs), pack2(f[2] * rs, f[3] * rs), pack2(f[4] * rs, f[5] * rs), pack2(f[6] * rs, f[7] * rs)};
      *(LAS v4u*)(L + (w >> 1) * 17408 + lane * 272 + ((w & 1) * 8 + i) * 16) = o4;
    }
    WG_BAR();
#pragma unroll
    for (int k = 0; k < 7; ++k) {
      const int kk = (w & 1) * 7 + k;
      st16p(R, OFF_XF + (unsigned)(((b - 224) * 4 + (w >> 1)) * 14 + kk) * 1024u, lane * 16, lds16(L, (w >> 1) * 17408 + (4 * kk + g) * 272 + rho * 16));
    }
    PST(1);
    if (b == 226) role_probe(R, wsb, L, w, lane);
  }
  PST(2);
}

struct MegaArgs { const unsigned short *x, *wg, *wu, *wd; unsigned short* out; char* ws; };

#define GET_POL() const unsigned pol_ = __builtin_amdgcn_readfirstlane(__builtin_amdgcn_raw_buffer_load_b32(R, CTL_POL, 0, 0)), pol0 = pol_ & 8192u, pol1 = (pol_ >> 1) & 8192u

struct Fetch {
  unsigned rdy, nxt, T; unsigned long long marks;
  DEV void refresh(rsrc_t R, int lane) { const unsigned c = ld4c(R, CTL_KV, (unsigned)(lane & 31) * 256u); rdy = (unsigned)__builtin_ctzll(~(__ballot(c >= 112u) & 0xFFFFFFFFull)); }
  DEV bool go(rsrc_t R, int lane, unsigned must, unsigned lim, bool first, unsigned& cap) {
    if (nxt > lim) return false;
    if ((nxt >> 1) >= rdy && first) {
      refresh(R, lane);
      for (unsigned n = 0; nxt <= must && (nxt >> 1) >= rdy; ++n) { if (n >= cap) { cap = 0; rdy = 32; break; } CFENCE(); refresh(R, lane); }
    }
    return (nxt >> 1) < rdy;
  }
  DEV void issued(unsigned n) { T += n; const unsigned sh = (nxt & 3u) * 16u; marks = (marks & ~(0xFFFFull << sh)) | ((unsigned long long)T << sh); ++nxt; }
  DEV unsigned since(unsigned seg) const { return T - (unsigned)((marks >> ((seg & 3u) * 16u)) & 0xFFFFull); }
};

#define MFMA "v_mfma_f32_16x16x32_bf16 "
#define CVT "v_cvt_pk_bf16_f32 "
#define ROR8 " row_ror:8 row_mask:0xf bank_mask:0xf\n\t"

DEV void role_v(rsrc_t R, LAS char* L, int v, int w, int lane) {
  const int g = lane >> 4, rho = lane & 15, hd = rho >> 3, r = rho & 7;
  constexpr unsigned L_M = 0, L_MP = 7168, L_RED = 11264, L_FLAG = 13312, L_T = 13376, L_D = 16384;
  TUNE_DECL;
  const unsigned lb = (unsigned)(size_t)L;
  const int pr = w < 4 ? 2 * w : 2 * w - 7;
  ST_DECL;
  v4u C[2][14];
#pragma unroll
  for (int a = 0; a < 2; ++a)
#pragma unroll
    for (int k = 0; k < 14; ++k) C[a][k] = ld16p(R, OFF_WF + (unsigned)((448 + (2 * pr + a) * 28 + v) * 14 + k) * 1024u, lane * 16);
  GET_POL();
  const unsigned mrd = L_M + (unsigned)(g * 128 + r * 16);
  auto mprime = [&]() {
    v4u mf[14];
#pragma unroll
    for (int k = 0; k < 14; ++k) mf[k] = lds16(L, mrd + k * 512);
    SB();
    f32x4 acc0 = {0.f, 0.f, 0.f, 0.f}, acc1 = acc0;
#pragma unroll
    for (int k = 0; k < 14; ++k) { acc0 = mma(mf[k], C[0][k], acc0); acc1 = mma(mf[k], C[1][k], acc1); }
    return pack8(acc0, acc1); };
  auto last = [&](v4u mp) { if (g < 2) st16c(R, OFF_MP + (unsigned)(((NSEG - 1) * 8 + pr) * 28 + v) * 512u, lane * 16, fixs4(mp)); };
  if (w < 4) {
    int bud = POLL_BUDGET;
    const v4u ks1 = onehot(0, r), ks2 = onehot(0, r ^ 4), ones = {0x3F803F80u, 0x3F803F80u, 0x3F803F80u, 0x3F803F80u};
    v4u sel;
    if (g < 2) sel = hd ? (v4u){0x0C0C0C0Cu, 0x0C0C0C0Cu, 0x03020100u, 0x07060504u} : (v4u){0x03020100u, 0x07060504u, 0x0C0C0C0Cu, 0x0C0C0C0Cu};
    else { const v4u o = onehot(0, ((r >> 2) == g - 2) ? 4 * hd + (r & 3) : -1);
#pragma unroll
      for (int q = 0; q < 4; ++q) sel[q] = (o[q] & 0xFFFFu) ? 0x0C0C0100u : (o[q] ? 0x01000C0Cu : 0x0C0C0C0Cu); }
    const unsigned red = lb + L_RED + (unsigned)(w * 512 + (g >> 1) * 256 + r * 32 + (g & 1) * 16);
    const unsigned eoff = (unsigned)((1 - hd) * 512 + (g * 8 + r) * 16), loff = (unsigned)(hd * 64 + r * 4);
    const unsigned m2 = w < 3 ? 1024u : 0u;
    __builtin_amdgcn_s_setprio(2);
    __builtin_amdgcn_s_waitcnt(0x0F70);
    WG_BAR();
    for (int s = 0;; ++s) {
      const unsigned bm = ho_blk(s, 0, pol0, pol1);
      v4u D[4];
#pragma unroll
      for (int i = 0; i < 4; ++i) D[i] = lds16(L, L_D + (s & 3) * 16384 + (4 * w + i) * 1024 + lane * 16);
      if (s) pace_wall(__builtin_amdgcn_readfirstlane(*(LAS unsigned*)(L + L_T)));
      { unsigned t;
        asm volatile(POLL_HEAD
          "buffer_load_dwordx4 v[232:235], %2, %3, %4 offen sc1\n\tbuffer_load_dwordx4 v[236:239], %2, %3, %5 offen sc1\n\t" POLL_BUD("%1")
          "s_waitcnt vmcnt(1)\n\tds_write_b128 %6, v[232:235]\n\tv_max3_u32 %0, v232, v233, v234\n\ts_waitcnt vmcnt(0)\n\tds_write_b128 %7, v[236:239]\n\t"
          "v_max3_u32 %0, %0, v235, v236\n\tv_max3_u32 %0, %0, v237, v238\n\tv_max_u32 %0, %0, v239\n\tv_cmp_eq_u32 vcc, -1, %0\n\ts_cbranch_vccnz 0b\n3:"
          : "=&v"(t), "+s"(bud)
          : "v"(lane * 16), "s"(R), "s"(bm + w * 2048), "s"(bm + w * 2048 + m2), "v"(lb + L_M + w * 2048 + lane * 16), "v"(lb + L_M + w * 2048 + m2 + lane * 16)
          : "memory", "vcc", "scc", "v232", "v233", "v234", "v235", "v236", "v237", "v238", "v239"); }
      ST_AT(0);
      WG_BAR();
      const unsigned tb = clk();
      v4u za[2], ef[2], sp[2]; unsigned lse[2];
      za[0] = mprime();
      if (s == NSEG - 1) { last(za[0]); break; }
      if (g < 2) *(LAS v4u*)(L + L_MP + pr * 512 + lane * 16) = za[0];
      ST_AT(1);
      const unsigned so = ho_blk(s, 1, pol0, pol1) + (unsigned)w * 2048u;
#define LOAD_E "buffer_load_dwordx4 v[232:235], %[eo], %[R], %[se] offen sc1\n\tbuffer_load_dwordx4 v[236:239], %[eo], %[R], %[se] offen offset:1024 sc1\n\t" \
               "buffer_load_dword v248, %[lo], %[R], %[sl] offen sc1\n\tbuffer_load_dword v249, %[lo], %[R], %[sl] offen offset:128 sc1\n\t"
#define E_OPS [eo] "v"(eoff), [lo] "v"(loff), [R] "s"(R), [se] "s"(ho_blk(s, 2, pol0, pol1) + (unsigned)w * 2048u), [sl] "s"(bm + 7168u + (unsigned)w * 256u)
      pace_clk(tb + T_E);
      asm volatile(LOAD_E : "=&{v[232:235]}"(ef[0]), "=&{v[236:239]}"(ef[1]), "=&{v248}"(lse[0]), "=&{v249}"(lse[1]) : E_OPS : "memory");
      pace_clk(tb + T_S);
      { unsigned t;
        asm volatile("buffer_load_dwordx4 v[240:243], %[vo], %[R], %[so] offen sc1\n\tbuffer_load_dwordx4 v[244:247], %[vo], %[R], %[so] offen offset:1024 sc1\n\t"
          "s_waitcnt vmcnt(2)\n\ts_branch 1f\n4:\n\ts_waitcnt vmcnt(0)\n\ts_branch 3f\n0:\n\t" LOAD_E POLL_BUD("%[bud]") "s_waitcnt vmcnt(0)\n1:\n\t"
          "v_max3_u32 %[t], v232, v233, v234\n\tv_max3_u32 %[t], %[t], v235, v236\n\tv_max3_u32 %[t], %[t], v237, v238\n\t"
          "v_max3_u32 %[t], %[t], v239, v248\n\tv_max_u32 %[t], %[t], v249\n\tv_cmp_eq_u32 vcc, -1, %[t]\n\ts_cbranch_vccnz 0b\n3:"
          : "+{v[232:235]}"(ef[0]), "+{v[236:239]}"(ef[1]), "+{v248}"(lse[0]), "+{v249}"(lse[1]), [t] "=&v"(t), [bud] "+s"(bud), "=&{v[240:243]}"(sp[0]), "=&{v[244:247]}"(sp[1])
          : E_OPS, [vo] "v"(lane * 16), [so] "s"(so) : "memory", "vcc", "scc"); }
      ST_AT(2);
      for (;;) {
        const unsigned f = *(LAS unsigned*)(L + L_FLAG + w * 4);
        CFENCE();
        za[1] = lds16(L, L_MP + (pr + 1) * 512 + (lane & 31) * 16);
        CFENCE();
        if (__builtin_expect(__builtin_amdgcn_readfirstlane(f) == (unsigned)s + 1u, 1)) break;
      }
#pragma unroll
      for (int a = 0; a < 2; ++a) {
        const f32x4 z = {0.f, 0.f, 0.f, 0.f};
        const v4u y = pack8(mma(ef[a], D[2 * a], z), mma(ror8v(ef[a]), D[2 * a + 1], z));
#pragma unroll
        for (int q = 0; q < 4; ++q) za[a][q] = g < 2 ? za[a][q] : y[q];
      }
      asm volatile("s_branch 1f\n4:\n\ts_waitcnt vmcnt(0)\n\ts_branch 3f\n0:\n\t"
        "buffer_load_dwordx4 v[240:243], %[vo], %[R], %[so] offen sc1\n\tbuffer_load_dwordx4 v[244:247], %[vo], %[R], %[so] offen offset:1024 sc1\n\t" POLL_BUD("%[bud]")
        "1:\n\ts_waitcnt vmcnt(0)\n\t"
        "v_max3_u32 v216, v240, v241, v242\n\t"
        "v_max3_u32 v217, v243, v244, v245\n\t"
        "v_max3_u32 v216, v216, v246, v247\n\t"
        MFMA "v[200:203], %[k1], v[240:243], 0\n\t"
        "v_max_u32 v216, v216, v217\n\t"
        MFMA "v[204:207], %[k2], v[240:243], 0\n\t"
        "v_cmp_eq_u32 vcc, -1, v216\n\t"
        "s_cbranch_vccnz 0b\n3:\n\t"
        MFMA "v[208:211], %[k1], v[244:247], 0\n\t"
        MFMA "v[212:215], %[k2], v[244:247], 0\n\t"
        "s_nop 1\n\t"
        "v_max3_f32 v216, v200, v201, v202\n\t"
        "v_cndmask_b32 v200, %[l0], v200, %[gm]\n\t"
        "v_max3_f32 v217, v203, v204, v205\n\t"
        "v_max3_f32 v218, v206, v207, %[l0]\n\t"
        "s_nop 0\n\t"
        "v_max3_f32 v216, v216, v217, v218\n\t"
        "v_max3_f32 v219, v208, v209, v210\n\t"
        "v_sub_f32 v200, v200, v216\n\tv_sub_f32 v201, v201, v216\n\tv_sub_f32 v202, v202, v216\n\tv_sub_f32 v203, v203, v216\n\t"
        "v_exp_f32 v200, v200\n\tv_exp_f32 v201, v201\n\tv_exp_f32 v202, v202\n\tv_exp_f32 v203, v203\n\t"
        "v_max3_f32 v220, v211, v212, v213\n\t"
        CVT "v217, v200, v201\n\t" CVT "v218, v202, v203\n\t"
        "v_max3_f32 v221, v214, v215, %[l1]\n\t"
        "v_perm_b32 v204, v218, v217, %[s0]\n\tv_perm_b32 v205, v218, v217, %[s1]\n\tv_perm_b32 v206, v218, v217, %[s2]\n\tv_perm_b32 v207, v218, v217, %[s3]\n\t"
        "v_cndmask_b32 v208, %[l1], v208, %[gm]\n\t"
        "v_max3_f32 v219, v219, v220, v221\n\t"
        MFMA "v[200:203], %[on], v[204:207], 0\n\t"
        "v_sub_f32 v208, v208, v219\n\tv_sub_f32 v209, v209, v219\n\t"
        MFMA "v[224:227], %[z0], v[204:207], 0\n\t"
        "v_sub_f32 v210, v210, v219\n\tv_sub_f32 v211, v211, v219\n\t"
        "v_exp_f32 v208, v208\n\tv_exp_f32 v209, v209\n\tv_exp_f32 v210, v210\n\tv_exp_f32 v211, v211\n\t"
        "v_rcp_f32 v216, v200\n\t"
        CVT "v217, v208, v209\n\t" CVT "v218, v210, v211\n\t"
        "v_mul_f32 v224, v224, v216\n\t"
        "v_perm_b32 v212, v218, v217, %[s0]\n\tv_perm_b32 v213, v218, v217, %[s1]\n\tv_perm_b32 v214, v218, v217, %[s2]\n\tv_perm_b32 v215, v218, v217, %[s3]\n\t"
        "v_mul_f32 v225, v225, v216\n\tv_mul_f32 v226, v226, v216\n\t"
        MFMA "v[208:211], %[on], v[212:215], 0\n\t"
        "v_mul_f32 v227, v227, v216\n\t"
        MFMA "v[228:231], %[z1], v[212:215], 0\n\t"
        "s_nop 5\n\t"
        "v_rcp_f32 v216, v208\n\t"
        "s_nop 0\n\t"
        "v_fma_f32 v224, v228, v216, v224\n\tv_fma_f32 v225, v229, v216, v225\n\tv_fma_f32 v226, v230, v216, v226\n\tv_fma_f32 v227, v231, v216, v227\n\t"
        "v_add_f32_dpp v224, v224, v224" ROR8 "v_add_f32_dpp v225, v225, v225" ROR8 "v_add_f32_dpp v226, v226, v226" ROR8 "v_add_f32_dpp v227, v227, v227" ROR8
        "ds_write_b128 %[red], v[224:227]\n\ts_waitcnt lgkmcnt(0)\n\ts_barrier"
        : [bud] "+s"(bud), "+{v[240:243]}"(sp[0]), "+{v[244:247]}"(sp[1])
        : [vo] "v"(lane * 16), [R] "s"(R), [so] "s"(so), [k1] "v"(ks1), [k2] "v"(ks2), [on] "v"(ones), [z0] "v"(za[0]), [z1] "v"(za[1]),
          [l0] "v"(lse[0]), [l1] "v"(lse[1]), [gm] "s"(0xFFFFFFFFull), [s0] "v"(sel.x), [s1] "v"(sel.y), [s2] "v"(sel.z), [s3] "v"(sel.w), [red] "v"(red)
        : "memory", "vcc", "scc", "v200", "v201", "v202", "v203", "v204", "v205", "v206", "v207", "v208", "v209", "v210", "v211", "v212", "v213", "v214", "v215",
          "v216", "v217", "v218", "v219", "v220", "v221", "v224", "v225", "v226", "v227", "v228", "v229", "v230", "v231");
      ST_AT(3);
      ST_END(s);
    }
  } else {
    unsigned cap = SPIN_CAP;
    Fetch F = {0u, 0u, 0u, 0ull};
    auto fetch = [&](unsigned must, unsigned lim) {
      for (int i = 0; i < 2 && F.go(R, lane, must, lim, i == 0, cap); ++i) {
#pragma unroll
        for (int h = 0; h < 8; ++h) dma<1, true>(R, lb + L_D + (F.nxt & 3u) * 16384u + ((w - 4) * 8 + h) * 1024, OFF_DX + (F.nxt * 448u + ((w - 4) * 8 + h) * 28 + v) * 1024u, lane * 16);
        F.issued(8);
      } };
    *(LAS unsigned*)(L + L_FLAG + (w - 4) * 4) = 0u;
    if (w < 6) fetch(0, 1);
    __builtin_amdgcn_s_waitcnt(0x0F70);
    WG_BAR();
    for (int s = 0;; ++s) {
      WG_BAR();
      const unsigned pa = ho_blk(s, 1, pol0, pol1) ^ 8192u;
      if (w == 7) {
        st4c(R, pa + v * 16, 0u, fixs(wall() + T_M));
        if (v == 0) st4c(R, CTL_P + s * 256, 0u, 1u); }
      else if (w < 6) fetch(0, s + 4 < NSEG - 2 ? s + 4 : NSEG - 2);
      const v4u mp = mprime();
      if (s == NSEG - 1) { last(mp); break; }
      if (g < 2) *(LAS v4u*)(L + L_MP + pr * 512 + lane * 16) = mp;
      CFENCE();
      *(LAS unsigned*)(L + L_FLAG + (w - 4) * 4) = (unsigned)s + 1u;
      ST_AT(1);
      if (w < 6) {
        if (F.nxt <= (unsigned)s + 1u && s + 1 <= NSEG - 2) fetch(s + 1, s + 1);
        const unsigned n = F.since(s + 1);
        if (n >= 24u) WAIT_VM(24); else if (n >= 16u) WAIT_VM(16); else if (n >= 8u) WAIT_VM(8); else WAIT_VM(0);
      } else if (w == 6) *(LAS unsigned*)(L + L_T) = pace_target(R, pa, lane) + T_L;
      ST_AT(2);
      WG_BAR();
      if (w == 7) {
        f32x2 t[4];
#pragma unroll
        for (int i = 0; i < 4; ++i) t[i] = lds8f(L, L_RED + i * 512 + lane * 8);
        const f32x2 u = (t[0] + t[1]) + (t[2] + t[3]);
        st4c(R, ho_blk(s + 1, 0, pol0, pol1) + v * 256, lane * 4, fixs(pack2(u[0], u[1])));
      } else if (w == 6) {
#pragma unroll
        for (int i = 0; i < 4; ++i) st16c(R, OFF_MP + (unsigned)((s * 8 + 2 * i) * 28 + v) * 512u, (lane >> 5) * (28 * 512) + (lane & 31) * 16, fixs4(lds16(L, L_MP + i * 1024 + lane * 16)));
      }
      ST_AT(3);
      ST_END(s);
    }
  }
  ST_DUMP();
}

DEV void role_q(rsrc_t R, LAS char* L, int h, int p, int w, int lane) {
  const int g = lane >> 4, rho = lane & 15, hd = rho >> 3, r = rho & 7;
  constexpr unsigned L_M = 0, L_RED = 7168, L_RED2 = 9216, L_X = 16384, L_DUMP = 49152;
  const unsigned lb = (unsigned)(size_t)L;
  ST_DECL;
  if (w < 7) {
    int bud = POLL_BUDGET;
    v4u A[7][2];
#pragma unroll
    for (int t = 0; t < 7; ++t)
#pragma unroll
      for (int u = 0; u < 2; ++u) A[t][u] = ld16p(R, OFF_WF + (unsigned)((h * 28 + (t < 6 ? 6 * p + t : 24 + p)) * 14 + 2 * w + u) * 1024u, lane * 16);
    GET_POL();
    const unsigned mo = (unsigned)(((2 * w + hd) * 4 + g) * 128 + r * 16);
    const unsigned mb = lb + L_M + (unsigned)((3 * p * 4 + g) * 128 + r * 16), mh = lb + L_M + (unsigned)(((12 + (p >> 1)) * 4 + g) * 128 + r * 16 + 8 * (p & 1));
    const unsigned red = lb + (g < 2 ? L_RED + (unsigned)(w * 256 + r * 32 + g * 16) : L_DUMP + (unsigned)(w * 1024 + lane * 16));
    v4u k0, k1, k2, kh = {0u, 0u, 0u, 0u}, bh = {0u, 0u, 0u, 0u};
    __builtin_amdgcn_s_waitcnt(0x0F70);
    for (int s = 0; s < NSEG; ++s) {
      asm volatile(POLL_HEAD
        "buffer_load_dwordx4 v[232:235], %[mo], %[R], %[so] offen sc1\n\t" POLL_BUD("%[bud]")
        "s_waitcnt vmcnt(0)\n\t"
        MFMA "v[100:103], %[a00], v[232:235], 0\n\t"
        "v_max_u32 v128, v234, v235\n\tv_max3_u32 v128, v232, v233, v128\n\t"
        MFMA "v[104:107], %[a10], v[232:235], 0\n\t"
        "v_cmp_eq_u32 vcc, -1, v128\n\ts_cbranch_vccnz 0b\n3:\n\t"
        "ds_write_b128 %[mw], v[232:235]\n\t"
        "v_mov_b32_dpp v236, v232" ROR8 "v_mov_b32_dpp v237, v233" ROR8
        MFMA "v[108:111], %[a20], v[232:235], 0\n\t"
        "v_mov_b32_dpp v238, v234" ROR8 "v_mov_b32_dpp v239, v235" ROR8
        MFMA "v[112:115], %[a30], v[232:235], 0\n\t"
        MFMA "v[100:103], %[a01], v[236:239], v[100:103]\n\t"
        MFMA "v[104:107], %[a11], v[236:239], v[104:107]\n\t"
        "s_waitcnt lgkmcnt(0)\n\ts_barrier\n\t"
        "ds_read_b128 v[128:131], %[mb]\n\tds_read_b128 v[132:135], %[mb] offset:512\n\tds_read_b64 v[156:157], %[mh]\n\tds_read_b128 v[136:139], %[mb] offset:1024\n\t"
        MFMA "v[108:111], %[a21], v[236:239], v[108:111]\n\t"
        MFMA "v[112:115], %[a31], v[236:239], v[112:115]\n\t"
        MFMA "v[116:119], %[a40], v[232:235], 0\n\t"
        CVT "v140, v100, v101\n\t" CVT "v141, v102, v103\n\t"
        MFMA "v[120:123], %[a50], v[232:235], 0\n\t"
        CVT "v142, v104, v105\n\t" CVT "v143, v106, v107\n\t"
        MFMA "v[116:119], %[a41], v[236:239], v[116:119]\n\t"
        CVT "v144, v108, v109\n\t" CVT "v145, v110, v111\n\t"
        MFMA "v[120:123], %[a51], v[236:239], v[120:123]\n\t"
        CVT "v146, v112, v113\n\t" CVT "v147, v114, v115\n\t"
        MFMA "v[124:127], %[a60], v[232:235], 0\n\t"
        "s_waitcnt lgkmcnt(3)\n\t"
        MFMA "v[100:103], v[140:143], v[128:131], 0\n\t"
        MFMA "v[124:127], %[a61], v[236:239], v[124:127]\n\t"
        CVT "v148, v116, v117\n\t" CVT "v149, v118, v119\n\t"
        "s_waitcnt lgkmcnt(0)\n\t"
        MFMA "v[100:103], v[144:147], v[132:135], v[100:103]\n\t"
        CVT "v150, v120, v121\n\t" CVT "v151, v122, v123\n\t"
        "s_nop 1\n\t"
        MFMA "v[100:103], v[148:151], v[136:139], v[100:103]\n\t"
        CVT "v152, v124, v125\n\t" CVT "v153, v126, v127\n\t"
        "s_nop 1\n\t"
        MFMA "v[100:103], v[152:155], v[156:159], v[100:103]\n\t"
        "s_nop 7\n\t"
        "ds_write_b128 %[red], v[100:103]\n\ts_waitcnt lgkmcnt(0)\n\ts_barrier"
        : [bud] "+s"(bud), "=&{v[140:143]}"(k0), "=&{v[144:147]}"(k1), "=&{v[148:151]}"(k2), "+{v[152:155]}"(kh), "+{v[156:159]}"(bh)
        : [mo] "v"(mo), [R] "s"(R), [so] "s"(ho_blk(s, 0, pol0, pol1)), [mw] "v"(lb + L_M + mo), [mb] "v"(mb), [mh] "v"(mh), [red] "v"(red),
          [a00] "v"(A[0][0]), [a01] "v"(A[0][1]), [a10] "v"(A[1][0]), [a11] "v"(A[1][1]), [a20] "v"(A[2][0]), [a21] "v"(A[2][1]), [a30] "v"(A[3][0]), [a31] "v"(A[3][1]),
          [a40] "v"(A[4][0]), [a41] "v"(A[4][1]), [a50] "v"(A[5][0]), [a51] "v"(A[5][1]), [a60] "v"(A[6][0]), [a61] "v"(A[6][1])
        : "memory", "vcc", "scc", "v100", "v101", "v102", "v103", "v104", "v105", "v106", "v107", "v108", "v109", "v110", "v111", "v112", "v113", "v114", "v115",
          "v116", "v117", "v118", "v119", "v120", "v121", "v122", "v123", "v124", "v125", "v126", "v127", "v128", "v129", "v130", "v131", "v132", "v133", "v134", "v135",
          "v136", "v137", "v138", "v139", "v232", "v233", "v234", "v235", "v236", "v237", "v238", "v239");
      ST_AT(2);
      const unsigned xb = L_X + (unsigned)(s & 3) * 8192u;
#pragma unroll
      for (int tt = 0; tt < 2; ++tt) {
        f32x4 sx = mma(k0, lds16(L, xb + tt * 4096 + lane * 16), (f32x4){0.f, 0.f, 0.f, 0.f});
        sx = mma(k1, lds16(L, xb + tt * 4096 + 1024 + lane * 16), sx);
        sx = mma(k2, lds16(L, xb + tt * 4096 + 2048 + lane * 16), sx);
        const v2u xh = lds8(L, xb + tt * 4096 + 3072 + lane * 16 + 8 * (p & 1));
        sx = mma(kh, (v4u){xh.x, xh.y, 0u, 0u}, sx);
        if (g < 2) *(LAS f32x4*)(L + L_RED2 + w * 1024 + (tt * 32 + rho * 2 + g) * 16) = sx;
      }
      if (w == 3 && s < NSEG - 1) pace_wall(pace_target(R, ho_blk(s, 1, pol0, pol1) ^ 8192u, lane));
      WG_BAR();
      ST_AT(3);
      ST_END(s);
    }
  } else {
    auto fetch = [&](int n) {
      const int sg = n < NSEG ? n : NSEG - 1;
#pragma unroll
      for (int tt = 0; tt < 2; ++tt) {
        dma<3, false>(R, lb + L_X + (n & 3) * 8192 + tt * 4096, OFF_XF + (unsigned)((sg * 2 + tt) * 14 + 3 * p) * 1024u, lane * 16);
        dma<1, false>(R, lb + L_X + (n & 3) * 8192 + tt * 4096 + 3072, OFF_XF + (unsigned)((sg * 2 + tt) * 14 + 12 + (p >> 1)) * 1024u, lane * 16);
      } };
    fetch(0); fetch(1); fetch(2);
    GET_POL();
    WAIT_VM(0);
    __builtin_amdgcn_s_setprio(2);
    for (int s = 0; s < NSEG; ++s) {
      const unsigned so = ho_blk(s, 1, pol0, pol1) + (unsigned)((h >> 1) * 1024 + p * 256 + (h & 1) * 128);
      WG_BAR();
      ST_AT(2);
      WG_BAR();
      { f32x2 t[7];
#pragma unroll
        for (int i = 0; i < 7; ++i) t[i] = lds8f(L, L_RED + i * 256 + (lane & 31) * 8);
        const f32x2 u = ((t[0] + t[1]) + (t[2] + t[3])) + ((t[4] + t[5]) + t[6]);
        st4c(R, so, (lane & 31) * 4, fixs(pack2(u[0], u[1])));
        ST_AT(0); }
      WG_BAR();
      { f32x4 t[7];
#pragma unroll
        for (int i = 0; i < 7; ++i) t[i] = lds16f(L, L_RED2 + i * 1024 + lane * 16);
        const f32x4 u = ((t[0] + t[1]) + (t[2] + t[3])) + ((t[4] + t[5]) + t[6]);
        st8c(R, OFF_SXM + (unsigned)((s * 16 + h) * 8 + p) * 256u, (lane >> 5) * 1024 + (lane & 31) * 8, (v2u){fixs(pack2(u[0], u[1])), fixs(pack2(u[2], u[3]))}); }
      fetch(s + 3);
      WAIT_VM(10);
      ST_AT(1);
      ST_END(s);
    }
  }
  ST_DUMP();
}

DEV void role_x(rsrc_t R, LAS char* L, int h, int w, int lane) {
  const int g = lane >> 4, rho = lane & 15, hd = rho >> 3, r = rho & 7;
  constexpr unsigned L_K = 7168;
  TUNE_DECL;
  GET_POL();
  const unsigned lb = (unsigned)(size_t)L;
  ST_DECL;
  if (w < 7) {
    int bud = POLL_BUDGET;
    const unsigned mo = (unsigned)(((2 * w + hd) * 4 + g) * 128 + r * 16);
    for (int s = 0; s < NSEG - 1; ++s) {
      WG_BAR();
      v4u kx[2][2];
#pragma unroll
      for (int i = 0; i < 4; ++i) kx[i >> 1][i & 1] = lds16(L, L_K + (s & 3) * 28672 + ((i >> 1) * 14 + 2 * w + (i & 1)) * 1024 + lane * 16);
      const v4u f0 = poll16(R, ho_blk(s, 0, pol0, pol1), mo, bud);
      ST_AT(0);
      const v4u f1 = ror8v(f0);
      f32x4 a0 = mma(kx[0][0], f0, (f32x4){0.f, 0.f, 0.f, 0.f}), a1 = mma(kx[1][1], f0, (f32x4){0.f, 0.f, 0.f, 0.f});
      a0 = mma(kx[0][1], f1, a0); a1 = mma(kx[1][0], f1, a1);
      f32x4 o;
#pragma unroll
      for (int i = 0; i < 4; ++i) o[i] = hd ? a1[i] : a0[i];
      *(LAS f32x4*)(L + w * 1024 + (r * 8 + ((g * 2 + hd) ^ r)) * 16) = o;
      WG_BAR();
      ST_AT(2);
      ST_END(s);
      if (w == 0 && s < NSEG - 2) { __builtin_amdgcn_s_sleep(14); pace_wall(pace_target(R, ho_blk(s, 1, pol0, pol1) ^ 8192u, lane) + T_X); }
    }
  } else {
    unsigned cap = SPIN_CAP;
    Fetch F = {0u, 0u, 0u, 0ull};
    auto fetch = [&](unsigned must, unsigned lim) {
      for (int i = 0; i < 2 && F.go(R, lane, must, lim, i == 0, cap); ++i) {
#pragma unroll
        for (int c = 0; c < 7; ++c) dma<4, true>(R, lb + L_K + (F.nxt & 3u) * 28672u + c * 4096, OFF_KX + ((F.nxt * 16u + h) * 28u + 4 * c) * 1024u, lane * 16);
        F.issued(28);
      } };
    fetch(0, 1);
    const int kg = (lane & 7) ^ (lane >> 3);
    const unsigned eo = (unsigned)(((kg >> 1) * 8 + (lane >> 3)) * 16 + (kg & 1) * 8), lo = (unsigned)(lane >> 3) * 4u;
    for (int s = 0; s < NSEG - 1; ++s) {
      const unsigned n = F.since(s);
      if (n >= 60u) WAIT_VM(60); else if (n >= 28u) WAIT_VM(28); else WAIT_VM(0);
      WG_BAR();
      ST_AT(1);
#define DPPX(op, d, ctl) "s_nop 1\n\t" op "_dpp " d ", " d ", " d " " ctl " row_mask:0xf bank_mask:0xf\n\t"
      asm volatile("s_waitcnt lgkmcnt(0)\n\ts_barrier\n\t"
        "ds_read_b128 v[100:103], %[la]\n\tds_read_b128 v[104:107], %[la] offset:1024\n\tds_read_b128 v[108:111], %[la] offset:2048\n\tds_read_b128 v[112:115], %[la] offset:3072\n\t"
        "ds_read_b128 v[116:119], %[la] offset:4096\n\tds_read_b128 v[120:123], %[la] offset:5120\n\tds_read_b128 v[124:127], %[la] offset:6144\n\t"
        "s_waitcnt lgkmcnt(5)\n\tv_pk_add_f32 v[100:101], v[100:101], v[104:105]\n\tv_pk_add_f32 v[102:103], v[102:103], v[106:107]\n\t"
        "s_waitcnt lgkmcnt(3)\n\tv_pk_add_f32 v[108:109], v[108:109], v[112:113]\n\tv_pk_add_f32 v[110:111], v[110:111], v[114:115]\n\t"
        "s_waitcnt lgkmcnt(1)\n\tv_pk_add_f32 v[116:117], v[116:117], v[120:121]\n\tv_pk_add_f32 v[118:119], v[118:119], v[122:123]\n\t"
        "v_pk_add_f32 v[100:101], v[100:101], v[108:109]\n\tv_pk_add_f32 v[102:103], v[102:103], v[110:111]\n\t"
        "s_waitcnt lgkmcnt(0)\n\tv_pk_add_f32 v[116:117], v[116:117], v[124:125]\n\tv_pk_add_f32 v[118:119], v[118:119], v[126:127]\n\t"
        "v_pk_add_f32 v[100:101], v[100:101], v[116:117]\n\tv_pk_add_f32 v[102:103], v[102:103], v[118:119]\n\t"
        "v_max_f32 v104, v100, v101\n\tv_max3_f32 v104, v102, v103, v104\n\t"
        DPPX("v_max_f32", "v104", "quad_perm:[1,0,3,2]") DPPX("v_max_f32", "v104", "quad_perm:[2,3,0,1]") DPPX("v_max_f32", "v104", "row_half_mirror")
        "v_sub_f32 v100, v100, v104\n\tv_sub_f32 v101, v101, v104\n\tv_sub_f32 v102, v102, v104\n\tv_sub_f32 v103, v103, v104\n\t"
        "v_exp_f32 v100, v100\n\tv_exp_f32 v101, v101\n\tv_exp_f32 v102, v102\n\tv_exp_f32 v103, v103\n\t"
        "v_add_f32 v105, v100, v101\n\tv_add_f32 v106, v102, v103\n\tv_add_f32 v105, v105, v106\n\t"
        DPPX("v_add_f32", "v105", "quad_perm:[1,0,3,2]") DPPX("v_add_f32", "v105", "quad_perm:[2,3,0,1]") DPPX("v_add_f32", "v105", "row_half_mirror")
        "v_rcp_f32 v106, v105\n\tv_log_f32 v105, v105\n\t"
        "v_mul_f32 v100, v100, v106\n\tv_mul_f32 v101, v101, v106\n\tv_mul_f32 v102, v102, v106\n\tv_mul_f32 v103, v103, v106\n\t"
        CVT "v100, v100, v101\n\t" CVT "v101, v102, v103\n\t"
        "v_add_f32 v104, v104, v105\n\t"
        "v_min_u32 v100, -2, v100\n\tv_min_u32 v101, -2, v101\n\tv_min_u32 v104, -2, v104\n\t"
        "buffer_store_dwordx2 v[100:101], %[eo], %[R], %[se] offen sc1\n\tbuffer_store_dword v104, %[lo], %[R], %[sl] offen sc1"
        :: [la] "v"(lb + lane * 16), [eo] "v"(eo), [lo] "v"(lo), [R] "s"(R), [se] "s"(ho_blk(s, 2, pol0, pol1) + (unsigned)h * 512u), [sl] "s"(ho_blk(s, 0, pol0, pol1) + 7168u + (unsigned)h * 64u)
        : "memory", "v100", "v101", "v102", "v103", "v104", "v105", "v106", "v107", "v108", "v109", "v110", "v111", "v112", "v113", "v114", "v115",
          "v116", "v117", "v118", "v119", "v120", "v121", "v122", "v123", "v124", "v125", "v126", "v127");
      ST_AT(0);
      F.T += 2;
      fetch(s + 1, s + 3 < NSEG - 2 ? s + 3 : NSEG - 2);
      ST_END(s);
    }
  }
  ST_DUMP();
}

template <int TYPE>
DEV void role_g(rsrc_t R, const MegaArgs& a, char* wsb, LAS char* L, int gi, int w, int lane) {
  const int wc = w & 3, ws = w >> 2, g = lane >> 4, rho = lane & 15, t0 = 8 * gi + 2 * wc;
  const unsigned lb = (unsigned)(size_t)L;
  v4u W[2][14];
  if (TYPE == 2) {
    const Tr T(lane, FF * 2);
    const rsrc_t RW = rsrc_of(ws ? a.wu : a.wg, DM * FF * 2);
#pragma unroll
    for (int t = 0; t < 14; ++t) T.load(RW, lb + ws * 57344 + wc * 14336, (unsigned)(wc * 112 * FF + 64 * (gi - 112)) * 2u, FF * 2, t);
    WAIT_VM(0);
    WG_BAR();
#pragma unroll
    for (int c = 0; c < 2; ++c)
#pragma unroll
      for (int k = 0; k < 14; ++k) W[c][k] = T.frag(L + c * 57344, k, wc);
    WG_BAR();
  }
  dma<4, false>(R, lb + w * 7168, OFF_XF + (unsigned)w * 7168u, lane * 16); dma<3, false>(R, lb + w * 7168 + 4096, OFF_XF + (unsigned)w * 7168u + 4096u, lane * 16);
  if (TYPE != 2) {
#pragma unroll
    for (int c = 0; c < 2; ++c)
#pragma unroll
      for (int k = 0; k < 14; ++k) W[c][k] = ld16p(R, OFF_WF + (unsigned)((t0 + c) * 14 + k) * 1024u, lane * 16);
  }
  __builtin_amdgcn_s_waitcnt(0x0F70);
  WG_BAR();
  const unsigned ctl = TYPE == 2 ? CTL_H : CTL_KV;
  for (int it = 0; it < 32; ++it) {
    LAS char* cur = L + (it & 1) * 57344 + ws * 28672 + lane * 16;
    if (it + 1 < 32) {
      const unsigned dst = lb + ((it + 1) & 1) * 57344 + w * 7168, src = OFF_XF + (unsigned)((it + 1) * 56 + w * 7) * 1024u;
      dma<4, false>(R, dst, src, lane * 16); dma<3, false>(R, dst + 4096, src + 4096u, lane * 16);
    }
    f32x4 acc[2][2];
#pragma unroll
    for (int c = 0; c < 2; ++c) acc[c][0] = acc[c][1] = (f32x4){0.f, 0.f, 0.f, 0.f};
#pragma unroll
    for (int k = 0; k < 14; ++k) {
      const v4u x0 = *(LAS v4u*)(cur + k * 1024), x1 = *(LAS v4u*)(cur + 14336 + k * 1024);
#pragma unroll
      for (int c = 0; c < 2; ++c) {
        if (TYPE == 1) { acc[c][0] = mma(x0, W[c][k], acc[c][0]); acc[c][1] = mma(x1, W[c][k], acc[c][1]); }
        else { acc[c][0] = mma(W[c][k], x0, acc[c][0]); acc[c][1] = mma(W[c][k], x1, acc[c][1]); }
      }
    }
    const int s = 2 * it + ws;
    if (TYPE == 0) {
      const int h = t0 / 28, u = (t0 % 28) >> 1;
#pragma unroll
      for (int tt = 0; tt < 2; ++tt) st16c(R, OFF_KX + (unsigned)(((s * 16 + h) * 2 + tt) * 14 + u) * 1024u, lane * 16, pack8(acc[0][tt], acc[1][tt]));
    } else if (TYPE == 1) {
#pragma unroll
      for (int c = 0; c < 2; ++c) st16c(R, OFF_DX + (unsigned)(s * 448 + (t0 - 448) + c) * 1024u, lane * 16, pack8(acc[c][0], acc[c][1]));
    } else {
      const int F = (t0 - 896) >> 1;
      const f32x4 ha = acc[0][0] * acc[1][0], hb = acc[0][1] * acc[1][1];
      const auto r0 = __builtin_amdgcn_permlane16_swap(pack2(ha[0], ha[1]), pack2(hb[0], hb[1]), false, false);
      const auto r1 = __builtin_amdgcn_permlane16_swap(pack2(ha[2], ha[3]), pack2(hb[2], hb[3]), false, false);
      st16c(R, OFF_HF + (unsigned)(s * 112 + (F >> 1)) * 1024u + (unsigned)(F & 1) * 512u, (g & 1) * 57344 + ((g >> 1) * 16 + rho) * 16, (v4u){r0[0], r1[0], r0[1], r1[1]});
    }
    if (it == 0 || it == 31) WAIT_VM(0); else if (TYPE == 2) WAIT_VM(1); else WAIT_VM(2);
    WG_BAR();
    if (w == 0 && lane == 0) {
      if (it == 0) signal(wsb, ctl);
      else if (it > 1) signal(wsb, ctl + (it - 1) * 256);
      if (it == 31) signal(wsb, ctl + 31 * 256);
    }
  }
}

DEV void role_f1(rsrc_t R, char* wsb, LAS char* L, int task, int w, int lane, unsigned& cap) {
  const int s = task >> 1, h = (task & 1) * 8 + w, g = lane >> 4, rho = lane & 15;
  if (w < 7) dma<4, false>(R, (unsigned)(size_t)L + w * 4096, OFF_XF + (unsigned)(s * 28 + w * 4) * 1024u, lane * 16);
  unsigned cnt = 0;
  wait_cnt(R, CTL_KV, lane, 32, cnt, s >> 1, 112, cap, true);
  v4u kx[28];
#pragma unroll
  for (int i = 0; i < 28; ++i) kx[i] = ld16c(R, OFF_KX + (unsigned)((s * 16 + h) * 28 + i) * 1024u, lane * 16);
  WAIT_VM(0);
  WG_BAR();
  f32x4 a00 = {0.f, 0.f, 0.f, 0.f}, a01 = a00, a11 = a00;
#pragma unroll
  for (int k = 0; k < 14; ++k) {
    const v4u x0 = lds16(L, k * 1024 + lane * 16), x1 = lds16(L, 14336 + k * 1024 + lane * 16);
    a00 = mma(kx[k], x0, a00); a01 = mma(kx[k], x1, a01); a11 = mma(kx[14 + k], x1, a11);
  }
  const float NEG = -3.0e38f;
#pragma unroll
  for (int i = 0; i < 4; ++i) { const bool ok = 4 * g + i <= rho; a00[i] = ok ? a00[i] : NEG; a11[i] = ok ? a11[i] : NEG; }
  const float m0 = x32max(x16max(fmaxf(fmaxf(a00[0], a00[1]), fmaxf(a00[2], a00[3]))));
  const float m1 = x32max(x16max(fmaxf(fmaxf(fmaxf(a01[0], a01[1]), fmaxf(a01[2], a01[3])), fmaxf(fmaxf(a11[0], a11[1]), fmaxf(a11[2], a11[3])))));
#pragma unroll
  for (int i = 0; i < 4; ++i) { a00[i] = ex2(a00[i] - m0); a01[i] = ex2(a01[i] - m1); a11[i] = ex2(a11[i] - m1); }
  const float l0 = x32add(x16add((a00[0] + a00[1]) + (a00[2] + a00[3])));
  const float l1 = x32add(x16add(((a01[0] + a01[1]) + (a01[2] + a01[3])) + ((a11[0] + a11[1]) + (a11[2] + a11[3]))));
  const float i0 = rcp(l0), i1 = rcp(l1);
  st16c(R, OFF_EX + (unsigned)((s * 16 + h) * 2) * 1024u, lane * 16, pack8(a00 * i0, (f32x4){0.f, 0.f, 0.f, 0.f}));
  st16c(R, OFF_EX + (unsigned)((s * 16 + h) * 2 + 1) * 1024u, lane * 16, pack8(a01 * i1, a11 * i1));
  if (g < 2) st4c(R, OFF_LX + (unsigned)(s * 16 + h) * 128u, lane * 4, __builtin_bit_cast(unsigned, g ? m1 + lg2(l1) : m0 + lg2(l0)));
  WAIT_VM(0);
  WG_BAR();
  if (w == 0 && lane == 0) signal(wsb, CTL_E + s * 256);
}

DEV v4u scale8(v4u e, float a) {
  v4u r;
#pragma unroll
  for (int q = 0; q < 4; ++q) r[q] = pack2(bflo(e[q]) * a, bfhi(e[q]) * a);
  return r;
}

DEV void role_f2(rsrc_t R, const MegaArgs& a, LAS char* L, int q, int grp, int w, int lane, unsigned& cap) {
  const int g = lane >> 4, rho = lane & 15;
  v4u Wd[7][4];
  { const Tr T(lane, DM * 2);
    const rsrc_t RW = rsrc_of(a.wd, DM * FF * 2);
#pragma unroll
    for (int r = 0; r < 2; ++r) {
#pragma unroll
      for (int t = 0; t < 16 - 4 * r; ++t) T.load(RW, (unsigned)(size_t)L + w * 16384, (unsigned)((224 * w + 128 * r) * DM + 64 * q) * 2u, DM * 2, t);
      WAIT_VM(0);
#pragma unroll
      for (int k = 0; k < 4 - r; ++k)
#pragma unroll
        for (int c = 0; c < 4; ++c) Wd[4 * r + k][c] = T.frag(L + w * 16384, k, c);
      asm volatile("s_waitcnt lgkmcnt(0)" ::: "memory");
    }
    WG_BAR(); }
  const v4u sel0 = onehot(0, rho < 8 ? rho : -1), sel1 = onehot(0, rho >= 8 ? rho - 8 : -1);
  for (int s = grp; s < NSEG; s += 21) {
    unsigned cnt = 0;
    wait_cnt(R, CTL_H, lane, 32, cnt, s >> 1, 28, cap, true);
    f32x4 acc[4][2];
#pragma unroll
    for (int c = 0; c < 4; ++c) acc[c][0] = acc[c][1] = (f32x4){0.f, 0.f, 0.f, 0.f};
    { v4u hf[14];
#pragma unroll
      for (int i = 0; i < 14; ++i) hf[i] = ld16c(R, OFF_HF + (unsigned)((s * 2 + i / 7) * 56 + 7 * w + i % 7) * 1024u, lane * 16);
#pragma unroll
      for (int k = 0; k < 7; ++k)
#pragma unroll
        for (int c = 0; c < 4; ++c) { acc[c][0] = mma(Wd[k][c], hf[k], acc[c][0]); acc[c][1] = mma(Wd[k][c], hf[7 + k], acc[c][1]); } }
    cnt = 0;
    wait_cnt(R, CTL_E, lane, 64, cnt, s, 2, cap, true);
    cnt = 0;
    wait_cnt(R, CTL_KV, lane, 32, cnt, s >> 1, 112, cap, true);
    v4u D[2][4], E[2][2]; float lse[2];
#pragma unroll
    for (int hh = 0; hh < 2; ++hh) {
      const unsigned sh = (unsigned)(s * 16 + 2 * w + hh);
#pragma unroll
      for (int c = 0; c < 4; ++c) D[hh][c] = ld16c(R, OFF_DX + (sh * 28 + 4 * q + c) * 1024u, lane * 16);
      E[hh][0] = ld16c(R, OFF_EX + sh * 2048u, lane * 16); E[hh][1] = ld16c(R, OFF_EX + sh * 2048u + 1024u, lane * 16);
      lse[hh] = __builtin_bit_cast(float, ld4c(R, OFF_LX + sh * 128u, ((g >> 1) * 16 + rho) * 4));
    }
    v4u sx[4], mp[4];
    if (w == 0) {
      for (unsigned n = 0; n < cap; ++n) {
        if (__builtin_amdgcn_readfirstlane(ld4c(R, CTL_P + (unsigned)(s <= 60 ? s + 3 : s < NSEG - 1 ? s + 1 : s) * 256u, 0u))) break;
        __builtin_amdgcn_s_sleep(8);
        CFENCE();
      }
    }
    WG_BAR();
    for (unsigned n = 0;; ++n) {
      unsigned m = 0;
#pragma unroll
      for (int i = 0; i < 4; ++i) {
        sx[i] = ld16c(R, OFF_SXM + (unsigned)((s * 16 + 2 * w) * 2 + i) * 1024u, lane * 16);
        mp[i] = ld16c(R, OFF_MP + (unsigned)((s * 8 + w) * 28 + 4 * q + i) * 512u, (lane & 31) * 16);
      }
#pragma unroll
      for (int i = 0; i < 4; ++i) m = umax(m, umax(max4u(sx[i]), max4u(mp[i])));
      if (__all(m != SENT)) break;
      if (n >= cap) { cap = 0; break; }
      __builtin_amdgcn_s_sleep(4);
      CFENCE();
    }
    v4u bm; float al[2][2];
#pragma unroll
    for (int hh = 0; hh < 2; ++hh) {
      f32x4 sv = mma(sel0, sx[2 * hh], (f32x4){0.f, 0.f, 0.f, 0.f});
      sv = mma(sel1, sx[2 * hh + 1], sv);
      const float mx = fmaxf(x16max(fmaxf(fmaxf(sv[0], sv[1]), fmaxf(sv[2], sv[3]))), lse[hh]);
      const float e0 = ex2(sv[0] - mx), e1 = ex2(sv[1] - mx), e2 = ex2(sv[2] - mx), e3 = ex2(sv[3] - mx), ea = ex2(lse[hh] - mx);
      const float inv = rcp(x16add((e0 + e1) + (e2 + e3)) + ea);
      bm[2 * hh] = pack2(e0 * inv, e1 * inv); bm[2 * hh + 1] = pack2(e2 * inv, e3 * inv);
      const unsigned au = __builtin_bit_cast(unsigned, ea * inv);
      const auto rr = __builtin_amdgcn_permlane32_swap(au, au, false, false);
      al[hh][0] = __builtin_bit_cast(float, (unsigned)rr[0]); al[hh][1] = __builtin_bit_cast(float, (unsigned)rr[1]);
    }
    const v4u zz = {0u, 0u, 0u, 0u};
    const v4u b0 = g < 2 ? bm : zz, b1 = g < 2 ? zz : bm;
#pragma unroll
    for (int c = 0; c < 4; ++c) { acc[c][0] = mma(mp[c], b0, acc[c][0]); acc[c][1] = mma(mp[c], b1, acc[c][1]); }
#pragma unroll
    for (int hh = 0; hh < 2; ++hh)
#pragma unroll
      for (int tt = 0; tt < 2; ++tt) {
        const v4u es = scale8(E[hh][tt], al[hh][tt]);
#pragma unroll
        for (int c = 0; c < 4; ++c) acc[c][tt] = mma(D[hh][c], es, acc[c][tt]);
      }
#pragma unroll
    for (int tt = 0; tt < 2; ++tt)
#pragma unroll
      for (int c = 0; c < 4; ++c) *(LAS f32x4*)(L + w * 8192 + (tt * 4 + c) * 1024 + lane * 16) = acc[c][tt];
    const size_t oi = (size_t)(s * 32 + (w >> 2) * 16 + rho) * DM + 64 * q + 16 * (w & 3) + 4 * g;
    const v2u xv = *(const GAS v2u*)(a.x + oi);
    WG_BAR();
    f32x4 t[8];
#pragma unroll
    for (int i = 0; i < 8; ++i) t[i] = lds16f(L, i * 8192 + w * 1024 + lane * 16);
    const f32x4 u = ((t[0] + t[1]) + (t[2] + t[3])) + ((t[4] + t[5]) + (t[6] + t[7]));
    *(GAS v2u*)(a.out + oi) = (v2u){pack2(u[0] + bflo(xv.x), u[1] + bfhi(xv.x)), pack2(u[2] + bflo(xv.y), u[3] + bfhi(xv.y))};
    WG_BAR();
  }
}

__global__ void __launch_bounds__(512, 2) mega_kernel(MegaArgs a) {
  extern __shared__ __attribute__((aligned(16))) char smem[];
  LAS char* L = (LAS char*)smem;
  const int b = blockIdx.x, w = __builtin_amdgcn_readfirstlane(threadIdx.x >> 6), lane = threadIdx.x & 63;
  char* const wsb = ws_base(a.ws);
  const rsrc_t R = __builtin_amdgcn_make_buffer_rsrc(wsb, 0, WS_USED, 0x00020000);
  const int side = (b >> 2) & 1, idx = (b >> 3) * 4 + (b & 3);
  if (side == 0 && idx < 108) {
    if (idx < 28) role_v(R, L, idx, w, lane);
    else if (idx < 44) role_x(R, L, idx - 28, w, lane);
    else role_q(R, L, (idx - 44) >> 2, (idx - 44) & 3, w, lane);
    return;
  }
  const int bi = side ? idx : 128 + idx - 108;
  ST_DECL;
  ST_AT(0);
  if (bi < 56) role_g<0>(R, a, wsb, L, bi, w, lane);
  else if (bi < 112) role_g<1>(R, a, wsb, L, bi, w, lane);
  else if (bi < 140) role_g<2>(R, a, wsb, L, bi, w, lane);
  ST_AT(1);
  unsigned cap = SPIN_CAP;
  if (bi < 128) role_f1(R, wsb, L, bi, w, lane, cap);
  ST_AT(2);
  if (bi < 147) role_f2(R, a, L, bi % 7, bi / 7, w, lane, cap);
  ST_AT(3);
  ST_END(0);
  ST_DUMP();
}

extern "C" void kernel_launch(void* const* d_in, const int* in_sizes, int n_in, void* d_out, int out_size, void* d_ws, size_t ws_size, hipStream_t stream) {
  ProArgs p;
  p.x = (const unsigned short*)d_in[0]; p.mem0 = (const unsigned short*)d_in[1];
  p.wq = (const unsigned short*)d_in[2]; p.wk = (const unsigned short*)d_in[3]; p.wv = (const unsigned short*)d_in[4]; p.wo = (const unsigned short*)d_in[5];
  p.ws = (char*)d_ws;
  (void)hipFuncSetAttribute((const void*)pro_kernel, hipFuncAttributeMaxDynamicSharedMemorySize, PRO_LDS);
  pro_kernel<<<PRO_GRID, 512, PRO_LDS, stream>>>(p);
  MegaArgs m; m.x = p.x;
  m.wg = (const unsigned short*)d_in[8]; m.wu = (const unsigned short*)d_in[9]; m.wd = (const unsigned short*)d_in[10]; m.out = (unsigned short*)d_out; m.ws = (char*)d_ws;
  (void)hipFuncSetAttribute((const void*)mega_kernel, hipFuncAttributeMaxDynamicSharedMemorySize, LDS_BYTES);
  mega_kernel<<<256, 512, LDS_BYTES, stream>>>(m);
}
```

```cpp
#include <hip/hip_runtime.h>
#include <hip/hip_bf16.h>
#include <type_traits>

typedef unsigned v4u __attribute__((ext_vector_type(4)));
typedef unsigned v2u __attribute__((ext_vector_type(2)));
typedef float f32x4 __attribute__((ext_vector_type(4)));
typedef float f32x2 __attribute__((ext_vector_type(2)));
typedef __bf16 bf16x8 __attribute__((ext_vector_type(8)));
typedef __bf16 bf16x2 __attribute__((ext_vector_type(2)));
typedef __amdgpu_buffer_rsrc_t rsrc_t;
#define LAS __attribute__((address_space(3)))
#define GAS __attribute__((address_space(1)))
#define DEV __device__ __forceinline__

constexpr int S_ = 2048, DM = 448, NH = 16, DH = 256, FF = 1792, NSEG = 64;
constexpr unsigned MB = 1u << 20;
constexpr unsigned OFF_HO = 0;
constexpr unsigned OFF_CTL = 4 * MB;
constexpr unsigned CTL_G = OFF_HO + 102 * 16384;
constexpr unsigned CTL_E = OFF_CTL;
constexpr unsigned CTL_P = OFF_CTL + 8192;
constexpr unsigned CTL_BYTES = 16384;
constexpr unsigned CTL_POL = OFF_CTL + CTL_BYTES;
constexpr unsigned OFF_DBG = 101 * MB;
constexpr unsigned OFF_SXM = 5 * MB;
constexpr unsigned OFF_MP = 7 * MB;
constexpr unsigned OFF_XF = 14 * MB;
constexpr unsigned OFF_EX = 18 * MB;
constexpr unsigned OFF_LX = 20 * MB;
constexpr unsigned OFF_HF = 21 * MB;
constexpr unsigned OFF_WF = 28 * MB;
constexpr unsigned OFF_KX = 44 * MB;
constexpr unsigned OFF_DX = 72 * MB;
constexpr unsigned WS_USED = 104 * MB;
constexpr unsigned SENT = 0xFFFFFFFFu;
constexpr unsigned SPIN_CAP = 1u << 21;
constexpr int POLL_BUDGET = 1 << 22;
#define BE "1"
#define BS "1"
#define BUD_DUMP() do {} while (0)
constexpr int LDS_BYTES = 163840;

DEV unsigned pack2(float lo, float hi) { f32x2 f = {lo, hi}; return __builtin_bit_cast(unsigned, __builtin_convertvector(f, bf16x2)); }
DEV v4u pack8(f32x4 a, f32x4 b) { return (v4u){pack2(a[0], a[1]), pack2(a[2], a[3]), pack2(b[0], b[1]), pack2(b[2], b[3])}; }
DEV unsigned fixs(unsigned x) { return x < 0xFFFFFFFEu ? x : 0xFFFFFFFEu; }
DEV v4u fixs4(v4u v) { return (v4u){fixs(v.x), fixs(v.y), fixs(v.z), fixs(v.w)}; }
DEV float bflo(unsigned u) { return __builtin_bit_cast(float, u << 16); }
DEV float bfhi(unsigned u) { return __builtin_bit_cast(float, u & 0xFFFF0000u); }
DEV f32x4 mma(v4u a, v4u b, f32x4 c) { return __builtin_amdgcn_mfma_f32_16x16x32_bf16(__builtin_bit_cast(bf16x8, a), __builtin_bit_cast(bf16x8, b), c, 0, 0, 0); }
DEV v4u ld16c(rsrc_t r, unsigned so, unsigned vo) { return __builtin_amdgcn_raw_buffer_load_b128(r, vo, so, 16); }
DEV unsigned ld4c(rsrc_t r, unsigned so, unsigned vo) { return __builtin_amdgcn_raw_buffer_load_b32(r, vo, so, 16); }
DEV v4u ld16p(rsrc_t r, unsigned so, unsigned vo) { return __builtin_amdgcn_raw_buffer_load_b128(r, vo, so, 0); }
DEV void st16c(rsrc_t r, unsigned so, unsigned vo, v4u v) { __builtin_amdgcn_raw_buffer_store_b128(v, r, vo + so, 0, 16); }
DEV void st8c(rsrc_t r, unsigned so, unsigned vo, v2u v) { __builtin_amdgcn_raw_buffer_store_b64(v, r, vo, so, 16); }
DEV void st4c(rsrc_t r, unsigned so, unsigned vo, unsigned v) { __builtin_amdgcn_raw_buffer_store_b32(v, r, vo, so, 16); }
DEV unsigned umax(unsigned a, unsigned b) { return a > b ? a : b; }
DEV unsigned max4u(v4u v) { return umax(umax(v.x, v.y), umax(v.z, v.w)); }
DEV unsigned ror8(unsigned x) { return __builtin_amdgcn_mov_dpp(x, 0x128, 0xf, 0xf, false); }
DEV float ror8f(float x) { return __builtin_bit_cast(float, ror8(__builtin_bit_cast(unsigned, x))); }
DEV v4u ror8v(v4u v) { return (v4u){ror8(v.x), ror8(v.y), ror8(v.z), ror8(v.w)}; }
DEV float x16max(float x) { unsigned u = __builtin_bit_cast(unsigned, x); auto r = __builtin_amdgcn_permlane16_swap(u, u, false, false); return fmaxf(__builtin_bit_cast(float, (unsigned)r[0]), __builtin_bit_cast(float, (unsigned)r[1])); }
DEV float x32max(float x) { unsigned u = __builtin_bit_cast(unsigned, x); auto r = __builtin_amdgcn_permlane32_swap(u, u, false, false); return fmaxf(__builtin_bit_cast(float, (unsigned)r[0]), __builtin_bit_cast(float, (unsigned)r[1])); }
DEV float x16add(float x) { unsigned u = __builtin_bit_cast(unsigned, x); auto r = __builtin_amdgcn_permlane16_swap(u, u, false, false); return __builtin_bit_cast(float, (unsigned)r[0]) + __builtin_bit_cast(float, (unsigned)r[1]); }
DEV float x32add(float x) { unsigned u = __builtin_bit_cast(unsigned, x); auto r = __builtin_amdgcn_permlane32_swap(u, u, false, false); return __builtin_bit_cast(float, (unsigned)r[0]) + __builtin_bit_cast(float, (unsigned)r[1]); }
DEV float ex2(float x) { return __builtin_amdgcn_exp2f(x); }
DEV float lg2(float x) { return __builtin_amdgcn_logf(x); }
DEV float rcp(float x) { return __builtin_amdgcn_rcpf(x); }
#define CFENCE() asm volatile("" ::: "memory")
#define WG_BAR() do { asm volatile("s_waitcnt lgkmcnt(0)" ::: "memory"); __builtin_amdgcn_s_barrier(); asm volatile("" ::: "memory"); } while (0)
#define WAIT_VM(n) asm volatile("s_waitcnt vmcnt(" #n ")" ::: "memory")
DEV v4u lds16(LAS char* l, unsigned off) { return *(LAS v4u*)(l + off); }
DEV v2u lds8(LAS char* l, unsigned off) { return *(LAS v2u*)(l + off); }
DEV f32x4 lds16f(LAS char* l, unsigned off) { return *(LAS f32x4*)(l + off); }
DEV f32x2 lds8f(LAS char* l, unsigned off) { return *(LAS f32x2*)(l + off); }
DEV v4u onehot(int g, int want) {
  v4u r; const int i = want - 8 * g;
#pragma unroll
  for (int q = 0; q < 4; ++q) r[q] = (i == 2 * q) ? 0x3F80u : ((i == 2 * q + 1) ? 0x3F800000u : 0u);
  return r;
}
DEV unsigned ho_blk(int s, int type, unsigned pol0, unsigned pol1) { return OFF_HO + (unsigned)(s >> 5) * (2 * MB) + (unsigned)((s & 31) * 3 + type) * 16384u + ((s >> 5) ? pol1 : pol0); }

#define ST_DECL do {} while (0)
#define ST_AT(k) do {} while (0)
#define ST_END(s) do {} while (0)
#define ST_DUMP() do {} while (0)
#define PST(k) do {} while (0)

#define DMA1(o, c) "buffer_load_dwordx4 %1, %2, %3 offen" o c " lds\n\t"
#define DMA_ASM(body) asm volatile("s_mov_b32 m0, %0\n\ts_nop 3\n\t" body :: "s"(dst), "v"(vo), "s"(R), "s"(so) : "memory")
template <int N, bool COH> DEV void dma(rsrc_t R, unsigned dst, unsigned so, unsigned vo) {
  if constexpr (COH) {
    if constexpr (N == 1) DMA_ASM(DMA1("", " sc1"));
    else if constexpr (N == 3) DMA_ASM(DMA1("", " sc1") DMA1(" offset:1024", " sc1") DMA1(" offset:2048", " sc1"));
    else DMA_ASM(DMA1("", " sc1") DMA1(" offset:1024", " sc1") DMA1(" offset:2048", " sc1") DMA1(" offset:3072", " sc1"));
  } else {
    if constexpr (N == 1) DMA_ASM(DMA1("", ""));
    else if constexpr (N == 3) DMA_ASM(DMA1("", "") DMA1(" offset:1024", "") DMA1(" offset:2048", ""));
    else DMA_ASM(DMA1("", "") DMA1(" offset:1024", "") DMA1(" offset:2048", "") DMA1(" offset:3072", ""));
  }
}

#define SB() __builtin_amdgcn_sched_barrier(0)
#define POLL_HEAD "s_branch 0f\n4:\n\ts_waitcnt vmcnt(0)\n\ts_branch 3f\n0:\n\t"
#define POLL_BUD(b) "s_sub_u32 " b ", " b ", 1\n\ts_cmp_lt_i32 " b ", 0\n\ts_cbranch_scc1 4b\n\t"
DEV v4u poll16(rsrc_t R, unsigned so, unsigned vo, int& bud) {
  v4u d; unsigned t;
  asm volatile(POLL_HEAD
    "buffer_load_dwordx4 v[232:235], %3, %4, %5 offen sc1\n\t" POLL_BUD("%2")
    "s_waitcnt vmcnt(0)\n\t"
    "v_max_u32 %1, v234, v235\n\tv_max3_u32 %1, v232, v233, %1\n\tv_cmp_eq_u32 vcc, -1, %1\n\ts_cbranch_vccnz 0b\n3:"
    : "=&{v[232:235]}"(d), "=&v"(t), "+s"(bud) : "v"(vo), "s"(R), "s"(so) : "memory", "vcc", "scc");
  return d;
}
DEV void wait_words(rsrc_t R, unsigned go, unsigned than, unsigned& cap) {
  for (unsigned n = 0;; ++n) {
    if (__all(ld4c(R, 0u, go) > than)) break;
    if (n >= cap) { cap = 0; break; }
    __builtin_amdgcn_s_sleep(8);
    CFENCE();
  }
  CFENCE();
}
DEV unsigned min16(unsigned c) {
  c = min(c, (unsigned)__builtin_amdgcn_mov_dpp(c, 0x128, 0xf, 0xf, false)); c = min(c, (unsigned)__builtin_amdgcn_mov_dpp(c, 0x124, 0xf, 0xf, false));
  c = min(c, (unsigned)__builtin_amdgcn_mov_dpp(c, 0x122, 0xf, 0xf, false)); return min(c, (unsigned)__builtin_amdgcn_mov_dpp(c, 0x121, 0xf, 0xf, false));
}
DEV unsigned ctl_g(int gi, unsigned pol0) { return CTL_G + (unsigned)((gi >> 6) * 16384 + (gi & 63) * 128) + pol0; }
DEV unsigned pa_blk(int s, unsigned pol0, unsigned pol1) { return OFF_HO + (unsigned)(s >> 5) * (2 * MB) + (unsigned)(96 + ((s >> 4) & 1)) * 16384u + (unsigned)(s & 15) * 512u + ((s >> 5) ? pol1 : pol0); }
DEV unsigned gw_head(int h, int lane, unsigned pol0) { const int a = (28 * h >> 3) + (lane & 7), z = (28 * h + 27) >> 3; return ctl_g(a < z ? a : z, pol0); }
#define TUNE_DECL constexpr int T_S = 1385, T_M = 116, T_E = 1050, T_L = 28, T_X = 0, T_P = 3, T_A = 100, T_C = 500; (void)T_A; (void)T_C; (void)T_P; (void)T_S; (void)T_M; (void)T_E; (void)T_L; (void)T_X
#define STR_(x) #x
#define PLACE(n) asm volatile(".p2alignl 8, 0xBF800000\n\t.fill " STR_(n) ", 4, 0xBF800000")
#define PLACE_(n) PLACE(n)
#define AL_V 16
#define AL_H 0
#define AL_Q 0
#define AL_R 0
#define AL_X 0
#define AL_Y 0
DEV unsigned clk() { return (unsigned)__builtin_amdgcn_s_memtime(); }
DEV unsigned wall() { return (unsigned)__builtin_amdgcn_s_memrealtime(); }
DEV unsigned pace_load(rsrc_t R, unsigned off, int lane, unsigned go) { return ld4c(R, 0u, lane < 32 ? off + (unsigned)(lane < 28 ? lane : 0) * 16u : go); }
DEV v2u pace_target(unsigned x, int lane) {
  const unsigned n = wall();
  int d = x == SENT ? 0 : (int)(x - n);
  d = d < 0 ? 0 : (d > 300 ? 300 : d);
  const unsigned m = min16(lane < 32 ? ~(unsigned)d : x);
  return (v2u){n + ~min((unsigned)__builtin_amdgcn_readlane(m, 0), (unsigned)__builtin_amdgcn_readlane(m, 16)), (unsigned)__builtin_amdgcn_readlane(m, 32)};
}
DEV void pace_wall(unsigned t) {
#define RD_WALL "s_memrealtime s[96:97]\n\ts_waitcnt lgkmcnt(0)\n\ts_sub_u32 s96, %0, s96\n\t"
#define REST(n) "s_cmp_lt_i32 s96, " n "\n\ts_cbranch_scc1 3f\n\ts_nop 15\n\t"
  asm volatile(RD_WALL "s_cmp_gt_i32 s96, 3\n\ts_cbranch_scc1 1f\n"
    "2:\n\ts_cmp_lt_i32 s96, 1\n\ts_cbranch_scc1 3f\n\t" RD_WALL "s_branch 2b\n"
    "1:\n\t" RD_WALL "s_cmp_gt_i32 s96, 3\n\ts_cbranch_scc1 1b\n\t" REST("1") REST("2") REST("3") "\n3:"
    :: "s"(t) : "memory", "scc", "s96", "s97");
}
DEV unsigned wall_fine() {
  unsigned r;
  asm volatile("s_memrealtime s[96:97]\n\ts_memtime s[94:95]\n\ts_waitcnt lgkmcnt(0)\n\ts_mov_b32 %0, s96\n"
    "1:\n\ts_memrealtime s[96:97]\n\ts_waitcnt lgkmcnt(0)\n\ts_cmp_eq_u32 s96, %0\n\ts_cbranch_scc1 1b\n\t"
    "s_mov_b32 %0, s96\n\ts_memtime s[96:97]\n\ts_waitcnt lgkmcnt(0)\n\ts_sub_u32 s96, s96, s94\n\ts_mul_i32 s96, s96, 47\n\ts_lshr_b32 s96, s96, 10\n\ts_sub_u32 %0, %0, s96"
    : "=&s"(r) :: "memory", "scc", "s94", "s95", "s96", "s97");
  return r;
}
#define PACE_CLK(d) "5:\n\ts_memtime s[96:97]\n\ts_waitcnt lgkmcnt(0)\n\ts_sub_u32 s96, s96, s98\n\ts_cmp_lt_u32 s96, " d "\n\ts_cbranch_scc1 5b\n\t"
DEV void signal(char* wsb, unsigned off) { __hip_atomic_fetch_add((GAS unsigned*)(wsb + off), 1u, __ATOMIC_RELAXED, __HIP_MEMORY_SCOPE_AGENT); }

struct ProArgs { const unsigned short *x, *mem0, *wq, *wk, *wv, *wo; char* ws; };
DEV char* ws_base(char* ws) { return (char*)(((size_t)ws + 2 * MB - 1) & ~(size_t)(2 * MB - 1)); }
DEV v4u gl16(const unsigned short* p) { return *(const GAS v4u*)p; }
DEV rsrc_t rsrc_of(const void* p, unsigned bytes) { return __builtin_amdgcn_make_buffer_rsrc((void*)p, 0, bytes, 0x00020000); }
template <int CTL> DEV float rorf(float x) { return __builtin_bit_cast(float, __builtin_amdgcn_mov_dpp(__builtin_bit_cast(unsigned, x), CTL, 0xf, 0xf, false)); }
DEV float wave_sum(float x) { x += rorf<0x128>(x); x += rorf<0x124>(x); x += rorf<0x122>(x); x += rorf<0x121>(x); return x32add(x16add(x)); }

struct Tr {
  unsigned src, rd;
  DEV Tr(int lane, unsigned ld) {
    src = (unsigned)(lane >> 3) * ld + (unsigned)((lane & 7) ^ ((lane >> 3) & 2)) * 16u;
    const int g = lane >> 4, q = (lane >> 2) & 3;
    rd = (unsigned)((8 * g + q) * 128 + (lane & 3) * 8 + (((q >> 1) | ((g & 1) << 1)) << 5));
  }
  DEV void load(rsrc_t R, unsigned dst, unsigned so, unsigned ld, int t) const { dma<1, false>(R, dst + t * 1024, so + t * 8 * ld, src ^ ((t & 1) << 6)); }
  DEV v4u frag(LAS char* base, int kk, int j) const {
    typedef short v4s __attribute__((ext_vector_type(4)));
    LAS char* p = base + (rd ^ (unsigned)(j << 5)) + kk * 4096;
    const v2u lo = __builtin_bit_cast(v2u, __builtin_amdgcn_ds_read_tr16_b64_v4i16((LAS v4s*)p)), hi = __builtin_bit_cast(v2u, __builtin_amdgcn_ds_read_tr16_b64_v4i16((LAS v4s*)(p + 512)));
    return (v4u){lo.x, lo.y, hi.x, hi.y};
  }
};

constexpr int PRO_GRID = 256, PRO_LDS = 131072;

__global__ void __launch_bounds__(512, 2) pro_kernel(ProArgs a) {
  extern __shared__ __attribute__((aligned(16))) char smem[];
  LAS char* L = (LAS char*)smem;
  const unsigned lb = (unsigned)(size_t)L;
  const int b = blockIdx.x, w = __builtin_amdgcn_readfirstlane(threadIdx.x >> 6), lane = threadIdx.x & 63, g = lane >> 4, rho = lane & 15;
  char* const wsb = ws_base(a.ws);
  const rsrc_t R = rsrc_of(wsb, WS_USED);
  ST_DECL;
  PST(0);
  const int isv = b >> 7, h = 2 * (b & 7) + ((b >> 6) & 1), rb = (b >> 5) & 1, cq = (b >> 3) & 3;
  constexpr unsigned L_B = 65536;
  const unsigned rsrc_off = (unsigned)g * 8192u + (unsigned)((rho ^ g) * 16);
  const Tr T(lane, DM * 2);
  const rsrc_t RB = rsrc_of(isv ? a.wo : a.wq, 448u * 8192u);
  v4u xr;
  asm volatile("buffer_load_dwordx4 %0, %1, %2, %3 offen" : "=&v"(xr) : "v"(lane < 56 ? lane * 16 : 0x7FFFFF00), "s"(rsrc_of(a.x, S_ * DM * 2)), "s"((8 * b + w) * DM * 2) : "memory");
  if (isv) {
#pragma unroll
    for (int i = 0; i < 8; ++i) T.load(RB, lb + L_B + (i & 1) * 32768, (unsigned)(h * 256 * DM + 112 * cq + 64 * (i & 1)) * 2u, DM * 2, 4 * w + (i >> 1));
  } else {
#pragma unroll
    for (int i = 0; i < 8; ++i) { const int t = 4 * w + (i >> 1);
      dma<1, false>(RB, lb + L_B + (i & 1) * 32768 + t * 1024, (unsigned)(32 * (7 * cq >> 1) + 4 * t) * 8192u + (unsigned)(h * 512 + (i & 1) * 256), rsrc_off ^ (((t >> 1) & 3) << 6)); }
  }
  auto norm = [&]() {
    float f[8];
#pragma unroll
    for (int q = 0; q < 4; ++q) { f[2 * q] = bflo(xr[q]); f[2 * q + 1] = bfhi(xr[q]); }
    const float mean = wave_sum(((f[0] + f[1]) + (f[2] + f[3])) + ((f[4] + f[5]) + (f[6] + f[7]))) * (1.f / DM);
    float vs = 0.f;
#pragma unroll
    for (int q = 0; q < 8; ++q) { f[q] -= mean; vs += f[q] * f[q]; }
    const float rs = 1.f / sqrtf(wave_sum(lane < 56 ? vs : 0.f) * (1.f / DM) + 1e-5f);
    if (lane < 56) *(LAS v4u*)(L + 57344 + lane * 144 + w * 16) = (v4u){pack2(f[0] * rs, f[1] * rs), pack2(f[2] * rs, f[3] * rs), pack2(f[4] * rs, f[5] * rs), pack2(f[6] * rs, f[7] * rs)}; };
  if (w == 7) {
    asm volatile("s_waitcnt vmcnt(8)" : "+v"(xr) :: "memory");
    norm();
    if (b >= 128 && b < 192) {
      st16c(R, b < 176 ? CTL_G + (unsigned)(b - 128) * 1024u : OFF_CTL + (unsigned)(b - 176) * 1024u, lane * 16, (v4u){0u, 0u, 0u, 0u});
    } else if (b >= 72 && b < 80) {
      const int pc = (b - 72) * 64 + lane;
      const v4u m = b < 79 ? fixs4(gl16(a.mem0 + (size_t)(pc & 7) * DM + (pc >> 3) * 8)) : (v4u){SENT, SENT, SENT, SENT};
      st16c(R, OFF_HO + (unsigned)(b - 72) * 1024u, lane * 16, m);
      st16c(R, OFF_HO + 8192u + (unsigned)(b - 72) * 1024u, lane * 16, m);
    }
    WAIT_VM(0); PST(1); WG_BAR();
#pragma unroll
    for (int k = 0; k < 7; ++k)
      st16c(R, OFF_XF + (unsigned)((b >> 1) * 14 + 2 * k) * 1024u, (unsigned)((lane >> 3) * 256 + ((b & 1) * 8 + (lane & 7)) * 16), lds16(L, 57344 + (k * 8 + (lane >> 3)) * 144 + (lane & 7) * 16));
    PST(2);
    if (b >= 16)
      for (unsigned c = b; c < 3568u; c += 240u)
        st16c(R, c < 1568u ? OFF_HO + c * 1024u : c < 3136u ? OFF_HO + 2 * MB + (c - 1568u) * 1024u : c < 3232u ? OFF_SXM + (61u * 32u - 3136u + c) * 1024u : OFF_MP + (61u * 112u - 3232u + c) * 1024u,
              lane * 16, (v4u){SENT, SENT, SENT, SENT});
    if (b < 12 && !(b & 4)) {
      unsigned best[2] = {~0u, ~0u};
      WAIT_VM(0);
      for (int i = 0; i < 4; ++i) {
        const char* p = wsb + OFF_HO + (size_t)(b >> 3) * (2 * MB) + (size_t)(100 + (b & 1)) * 16384 + (i & 1) * 8192 + (b & 2) * 2048 + (i >> 1) * 1280;
        unsigned t;
        const unsigned long long c0 = __builtin_amdgcn_s_memtime();
        asm volatile("s_waitcnt lgkmcnt(0)\n\tglobal_load_dword %0, %1, off sc1\n\ts_waitcnt vmcnt(0)" : "=&v"(t) : "v"(p) : "memory");
        const unsigned d = (unsigned)(__builtin_amdgcn_s_memtime() - c0);
        if (i & 1) best[1] = d < best[1] ? d : best[1]; else best[0] = d < best[0] ? d : best[0];
      }
      st8c(R, CTL_POL + (unsigned)((b >> 3) * 4 + (b & 3)) * 8u, 0u, (v2u){best[0], best[1]});
    }
    PST(3);
    return;
  }
  const rsrc_t RA = rsrc_of(isv ? a.wv : a.wk, 448u * 8192u);
  const unsigned sa = (unsigned)(224 * rb + 32 * w) * 8192u + (unsigned)h * 512u;
  v4u a1[8];
#pragma unroll
  for (int t = 0; t < 8; ++t) dma<1, false>(RA, lb + w * 8192 + t * 1024, sa + t * 32768u, rsrc_off ^ (((t >> 1) & 3) << 6));
#pragma unroll
  for (int t = 0; t < 8; ++t) a1[t] = ld16p(RA, sa + t * 32768u + 256u, rsrc_off ^ (((t >> 1) & 3) << 6));
  asm volatile("s_waitcnt vmcnt(24)" : "+v"(xr) :: "memory");
  norm();
  f32x4 acc[2][7];
#pragma unroll
  for (int j = 0; j < 7; ++j) acc[0][j] = acc[1][j] = (f32x4){0.f, 0.f, 0.f, 0.f};
  const unsigned rdo = (unsigned)((8 * (rho >> 2) + (rho & 3)) * 256 + ((g ^ rho) * 16));
  auto half = [&](int hf, auto mode) {
    v4u A[2][2], B[2][7];
    auto rd = [&](int es) {
#pragma unroll
      for (int i = 0; i < 2; ++i) A[es & 1][i] = lds16(L, w * 8192 + (rdo ^ (es << 6)) + i * 1024);
#pragma unroll
      for (int j = 0; j < 7; ++j)
        B[es & 1][j] = mode == 2 ? T.frag(L + L_B + (j >> 2) * 32768, 4 * hf + es, j & 3) : lds16(L, L_B + hf * 32768 + (rdo ^ (es << 6)) + ((j + mode) >> 1) * 8192 + ((j + mode) & 1) * 1024); };
    rd(0);
#pragma unroll
    for (int es = 0; es < 4; ++es) {
      if (es < 3) rd(es + 1);
      SB();
#pragma unroll
      for (int j = 0; j < 7; ++j) { acc[0][j] = mma(A[es & 1][0], B[es & 1][j], acc[0][j]); acc[1][j] = mma(A[es & 1][1], B[es & 1][j], acc[1][j]); }
      SB();
    } };
  auto both = [&](auto mode) {
    half(0, mode);
#pragma unroll
    for (int t = 0; t < 8; ++t) *(LAS v4u*)(L + w * 8192 + t * 1024 + lane * 16) = a1[t];
    PST(2);
    half(1, mode); };
  WAIT_VM(8); PST(1); WG_BAR();
  if (isv) both(std::integral_constant<int, 2>()); else if (cq & 1) both(std::integral_constant<int, 1>()); else both(std::integral_constant<int, 0>());
  const float sc = isv ? 1.f : 0.0625f * 1.4426950408889634f;
#pragma unroll
  for (int j = 0; j < 7; ++j) st16c(R, OFF_WF + (unsigned)((isv * 448 + h * 28 + 7 * cq + j) * 14 + 7 * rb + w) * 1024u, lane * 16, pack8(acc[0][j] * sc, acc[1][j] * sc));
  PST(3);
}

struct MegaArgs { const unsigned short *x, *wg, *wu, *wd; unsigned short* out; char* ws; };

DEV unsigned get_pol(rsrc_t R, int lane) {
  v2u t = __builtin_amdgcn_raw_buffer_load_b64(R, (lane & 7) * 8, CTL_POL, 0);
#pragma unroll
  for (int q = 0; q < 2; ++q) { t[q] = min(t[q], (unsigned)__builtin_amdgcn_mov_dpp(t[q], 0xB1, 0xf, 0xf, false)); t[q] = min(t[q], (unsigned)__builtin_amdgcn_mov_dpp(t[q], 0x4E, 0xf, 0xf, false)); }
  const unsigned long long m = __ballot(t.y < t.x);
  return (unsigned)((m & 1) << 13 | (m & 16) << 10);
}
#define GET_POL() const unsigned pol_ = get_pol(R, lane), pol0 = pol_ & 8192u, pol1 = (pol_ >> 1) & 8192u

struct Fetch {
  unsigned rdy, nxt, T; unsigned long long marks;
  DEV void seen(unsigned r) { r = __builtin_amdgcn_readfirstlane(r); rdy = r > rdy ? r : rdy; }
  DEV bool go(rsrc_t R, unsigned gw, int must, unsigned lim, unsigned& cap) {
    if (nxt > lim) return false;
    for (unsigned n = 0; (int)nxt <= must && (nxt >> 1) >= rdy; ++n) {
      if (n >= cap) { cap = 0; rdy = 32; break; }
      if (n) __builtin_amdgcn_s_sleep(2);
      CFENCE();
      seen(min16(ld4c(R, 0u, gw)));
    }
    return (nxt >> 1) < rdy;
  }
  DEV void issued(unsigned n) { T += n; const unsigned sh = (nxt & 3u) * 16u; marks = (marks & ~(0xFFFFull << sh)) | ((unsigned long long)T << sh); ++nxt; }
  DEV unsigned since(unsigned seg) const { return T - (unsigned)((marks >> ((seg & 3u) * 16u)) & 0xFFFFull); }
};

#define MFMA "v_mfma_f32_16x16x32_bf16 "
#define CVT "v_cvt_pk_bf16_f32 "
#define ROR8 " row_ror:8 row_mask:0xf bank_mask:0xf\n\t"

DEV void role_v(rsrc_t R, LAS char* L, int v, int w, int lane) {
  const int g = lane >> 4, rho = lane & 15, hd = rho >> 3, r = rho & 7;
  constexpr unsigned L_M = 0, L_MP = 7168, L_RED = 11264, L_FLAG = 13312, L_T = 13376, L_D = 16384;
  TUNE_DECL;
  const unsigned lb = (unsigned)(size_t)L;
  const int pr = w < 4 ? 2 * w : 2 * w - 7;
  ST_DECL;
  v4u C[2][14];
#pragma unroll
  for (int a = 0; a < 2; ++a)
#pragma unroll
    for (int k = 0; k < 14; ++k) C[a][k] = ld16p(R, OFF_WF + (unsigned)((448 + (2 * pr + a) * 28 + v) * 14 + k) * 1024u, lane * 16);
  GET_POL();
  const unsigned mrd = L_M + (unsigned)(g * 128 + r * 16);
  auto mprime = [&](auto mid) {
    v4u mf[14];
#pragma unroll
    for (int k = 0; k < 14; ++k) mf[k] = lds16(L, mrd + k * 512);
    SB();
    f32x4 acc0 = {0.f, 0.f, 0.f, 0.f}, acc1 = acc0;
#pragma unroll
    for (int k = 0; k < 14; ++k) { acc0 = mma(mf[k], C[0][k], acc0); acc1 = mma(mf[k], C[1][k], acc1); if (k == 5) mid(); }
    return pack8(acc0, acc1); };
  auto last = [&](v4u mp) { if (g < 2) st16c(R, OFF_MP + (unsigned)(((NSEG - 1) * 8 + pr) * 28 + v) * 512u, lane * 16, fixs4(mp)); };
  if (w < 4) {
    int bud = POLL_BUDGET;
    const v4u ks1 = onehot(0, r), ks2 = onehot(0, r ^ 4);
    v4u ones = {0x3F803F80u, 0x3F803F80u, 0x3F803F80u, 0x3F803F80u};
    asm volatile("" : "+v"(ones));
    v4u sel;
    if (g < 2) sel = hd ? (v4u){0x0C0C0C0Cu, 0x0C0C0C0Cu, 0x03020100u, 0x07060504u} : (v4u){0x03020100u, 0x07060504u, 0x0C0C0C0Cu, 0x0C0C0C0Cu};
    else { const v4u o = onehot(0, ((r >> 2) == g - 2) ? 4 * hd + (r & 3) : -1);
#pragma unroll
      for (int q = 0; q < 4; ++q) sel[q] = (o[q] & 0xFFFFu) ? 0x0C0C0100u : (o[q] ? 0x01000C0Cu : 0x0C0C0C0Cu); }
    const unsigned red = lb + L_RED + (unsigned)(w * 512 + (g >> 1) * 256 + r * 32 + (g & 1) * 16);
    const unsigned eoff = (unsigned)((1 - hd) * 512 + (g * 8 + r) * 16), loff = (unsigned)(hd * 64 + r * 4);
    const unsigned m2 = w < 3 ? 1024u : 0u;
    __builtin_amdgcn_s_setprio(2);
    __builtin_amdgcn_s_waitcnt(0x0F70);
    ST_AT(0);
    WG_BAR();
    ST_AT(1);
    ST_END(63);
    PLACE_(AL_V);
    for (int s = 0;; ++s) {
      const unsigned bm = ho_blk(s, 0, pol0, pol1);
      v4u D[4];
#pragma unroll
      for (int i = 0; i < 4; ++i) D[i] = lds16(L, L_D + (s & 3) * 16384 + (4 * w + i) * 1024 + lane * 16);
      if (s) pace_wall(__builtin_amdgcn_readfirstlane(*(LAS unsigned*)(L + L_T)));
      { unsigned t;
        asm volatile(POLL_HEAD
          "buffer_load_dwordx4 v[232:235], %2, %3, %4 offen sc1\n\tbuffer_load_dwordx4 v[236:239], %2, %3, %5 offen sc1\n\t" POLL_BUD("%1")
          "s_waitcnt vmcnt(1)\n\tds_write_b128 %6, v[232:235]\n\tv_max3_u32 %0, v232, v233, v234\n\ts_waitcnt vmcnt(0)\n\tds_write_b128 %7, v[236:239]\n\t"
          "v_max3_u32 %0, %0, v235, v236\n\tv_max3_u32 %0, %0, v237, v238\n\tv_max_u32 %0, %0, v239\n\tv_cmp_eq_u32 vcc, -1, %0\n\ts_cbranch_vccnz 0b\n3:"
          : "=&v"(t), "+s"(bud)
          : "v"(lane * 16), "s"(R), "s"(bm + w * 2048), "s"(bm + w * 2048 + m2), "v"(lb + L_M + w * 2048 + lane * 16), "v"(lb + L_M + w * 2048 + m2 + lane * 16)
          : "memory", "vcc", "scc", "v232", "v233", "v234", "v235", "v236", "v237", "v238", "v239"); }
      ST_AT(0);
      unsigned long long tb;
      asm volatile("s_waitcnt lgkmcnt(0)\n\ts_barrier\n\ts_memtime s[98:99]" : "=&{s[98:99]}"(tb) :: "memory");
      v4u za = mprime([] {});
      if (s == NSEG - 1) { last(za); BUD_DUMP(); break; }
      if (g < 2) *(LAS v4u*)(L + L_MP + pr * 512 + lane * 16) = za;
      ST_AT(1);
#define LOAD_E "buffer_load_dwordx4 v[232:235], %[eo], %[R], %[se] offen sc1\n\tbuffer_load_dwordx4 v[236:239], %[eo], %[R], %[se] offen offset:1024 sc1\n\t" \
               "buffer_load_dword v248, %[lo], %[R], %[sl] offen sc1\n\tbuffer_load_dword v249, %[lo], %[R], %[sl] offen offset:128 sc1\n\t"
#define LOAD_S "buffer_load_dwordx4 v[240:243], %[vo], %[R], %[so] offen sc1\n\tbuffer_load_dwordx4 v[244:247], %[vo], %[R], %[so] offen offset:1024 sc1\n\t"
#define READ_Z "ds_read_b32 v222, %[fa]\n\ts_mov_b64 exec, %[gm]\n\tds_read_b128 v[196:199], %[za]\n\t"
#define BUD_TO(l, n) "s_sub_u32 %[bud], %[bud], " n "\n\ts_cmp_lt_i32 %[bud], 0\n\ts_waitcnt vmcnt(0)\n\ts_cbranch_scc1 " l "\n\t"
      asm volatile(PACE_CLK("%[te]") LOAD_E PACE_CLK("%[ts]") LOAD_S
        "s_branch 1f\n"
        "6:\n\t" LOAD_S BUD_TO("3f", BS) "s_branch 2f\n"
        "7:\n\t" READ_Z "s_mov_b64 exec, -1\n\ts_waitcnt lgkmcnt(0)\n\tv_cmp_ne_u32 vcc, %[st], v222\n\ts_cbranch_vccnz 7b\n\ts_branch 8f\n"
        "0:\n\t" LOAD_E BUD_TO("4f", BE)
        "1:\n\ts_waitcnt vmcnt(5)\n\t"
        MFMA "v[200:203], v[232:235], %[d0], 0\n\t"
        "v_mov_b32_dpp v216, v232" ROR8 "v_mov_b32_dpp v217, v233" ROR8 "v_mov_b32_dpp v218, v234" ROR8 "v_mov_b32_dpp v219, v235" ROR8
        "s_waitcnt vmcnt(4)\n\t"
        MFMA "v[208:211], v[236:239], %[d2], 0\n\t"
        "v_mov_b32_dpp v224, v236" ROR8 "v_mov_b32_dpp v225, v237" ROR8
        MFMA "v[204:207], v[216:219], %[d1], 0\n\t"
        "v_mov_b32_dpp v226, v238" ROR8 "v_mov_b32_dpp v227, v239" ROR8
        "v_max3_u32 v220, v232, v233, v234\n\tv_max3_u32 v221, v235, v236, v237\n\t"
        MFMA "v[212:215], v[224:227], %[d3], 0\n\t"
        "v_max3_u32 v220, v220, v238, v239\n\ts_waitcnt vmcnt(2)\n\tv_max3_u32 v221, v221, v248, v249\n\tv_max_u32 v220, v220, v221\n\t"
        "v_cmp_eq_u32 vcc, -1, v220\n\ts_cbranch_vccnz 0b\n4:\n\t"
        READ_Z "s_not_b64 exec, exec\n\t"
        CVT "v192, v200, v201\n\t" CVT "v193, v202, v203\n\t" CVT "v196, v208, v209\n\t" CVT "v197, v210, v211\n\t"
        CVT "v194, v204, v205\n\t" CVT "v195, v206, v207\n\t" CVT "v198, v212, v213\n\t" CVT "v199, v214, v215\n\t"
        "s_mov_b64 exec, -1\n\t"
        "s_waitcnt vmcnt(0)\n2:\n\t"
        MFMA "v[200:203], %[k1], v[240:243], 0\n\t"
        "v_max3_u32 v216, v240, v241, v242\n\t"
        "v_max3_u32 v217, v243, v244, v245\n\t"
        MFMA "v[204:207], %[k2], v[240:243], 0\n\t"
        "v_max3_u32 v216, v216, v246, v247\n\t"
        "v_max_u32 v216, v216, v217\n\t"
        "v_cmp_eq_u32 vcc, -1, v216\n\t"
        "s_cbranch_vccnz 6b\n3:\n\t"
        MFMA "v[208:211], %[k1], v[244:247], 0\n\t"
        MFMA "v[212:215], %[k2], v[244:247], 0\n\t"
        "v_max3_f32 v216, v200, v201, v202\n\t"
        "v_cndmask_b32 v200, v248, v200, %[gm]\n\t"
        "v_max3_f32 v217, v203, v204, v205\n\t"
        "v_max3_f32 v218, v206, v207, v248\n\t"
        "v_max3_f32 v216, v216, v217, v218\n\t"
        "v_sub_f32 v200, v200, v216\n\tv_sub_f32 v201, v201, v216\n\tv_sub_f32 v202, v202, v216\n\tv_sub_f32 v203, v203, v216\n\t"
        "v_exp_f32 v200, v200\n\tv_exp_f32 v201, v201\n\tv_exp_f32 v202, v202\n\tv_exp_f32 v203, v203\n\t"
        "v_max3_f32 v219, v208, v209, v210\n\t"
        "v_max3_f32 v220, v211, v212, v213\n\t"
        CVT "v217, v200, v201\n\t" CVT "v218, v202, v203\n\t"
        "v_max3_f32 v221, v214, v215, v249\n\t"
        "v_perm_b32 v204, v218, v217, %[s0]\n\tv_perm_b32 v205, v218, v217, %[s1]\n\tv_perm_b32 v206, v218, v217, %[s2]\n\tv_perm_b32 v207, v218, v217, %[s3]\n\t"
        "v_cndmask_b32 v208, v249, v208, %[gm]\n\t"
        "v_max3_f32 v219, v219, v220, v221\n\t"
        MFMA "v[200:203], %[on], v[204:207], 0\n\t"
        "v_sub_f32 v208, v208, v219\n\tv_sub_f32 v209, v209, v219\n\t"
        MFMA "v[224:227], v[192:195], v[204:207], 0\n\t"
        "v_sub_f32 v210, v210, v219\n\tv_sub_f32 v211, v211, v219\n\t"
        "v_exp_f32 v208, v208\n\tv_exp_f32 v209, v209\n\tv_exp_f32 v210, v210\n\tv_exp_f32 v211, v211\n\t"
        "s_waitcnt lgkmcnt(0)\n\tv_cmp_ne_u32 vcc, %[st], v222\n\t"
        "v_rcp_f32 v216, v200\n\t"
        "s_cbranch_vccnz 7b\n8:\n\t"
        CVT "v217, v208, v209\n\t" CVT "v218, v210, v211\n\t"
        "v_mul_f32 v224, v224, v216\n\t"
        "v_perm_b32 v212, v218, v217, %[s0]\n\tv_perm_b32 v213, v218, v217, %[s1]\n\tv_perm_b32 v214, v218, v217, %[s2]\n\tv_perm_b32 v215, v218, v217, %[s3]\n\t"
        "v_mul_f32 v225, v225, v216\n\tv_mul_f32 v226, v226, v216\n\t"
        MFMA "v[208:211], %[on], v[212:215], 0\n\t"
        "v_mul_f32 v227, v227, v216\n\t"
        MFMA "v[228:231], v[196:199], v[212:215], 0\n\t"
        "s_nop 5\n\t"
        "v_rcp_f32 v216, v208\n\t"
        "s_nop 0\n\t"
        "v_fma_f32 v224, v228, v216, v224\n\tv_fma_f32 v225, v229, v216, v225\n\tv_fma_f32 v226, v230, v216, v226\n\tv_fma_f32 v227, v231, v216, v227\n\t"
        "v_add_f32_dpp v224, v224, v224" ROR8 "v_add_f32_dpp v225, v225, v225" ROR8 "v_add_f32_dpp v226, v226, v226" ROR8 "v_add_f32_dpp v227, v227, v227" ROR8
        "ds_write_b128 %[red], v[224:227]\n\ts_waitcnt lgkmcnt(0)\n\ts_barrier"
        : [bud] "+s"(bud), "+{v[192:195]}"(za), "+{s[98:99]}"(tb)
        : [vo] "v"(lane * 16), [R] "s"(R), [so] "s"(ho_blk(s, 1, pol0, pol1) + (unsigned)w * 2048u), [k1] "v"(ks1), [k2] "v"(ks2), [on] "v"(ones),
          [gm] "s"(0xFFFFFFFFull), [s0] "v"(sel.x), [s1] "v"(sel.y), [s2] "v"(sel.z), [s3] "v"(sel.w), [red] "v"(red),
          [eo] "v"(eoff), [lo] "v"(loff), [se] "s"(ho_blk(s, 2, pol0, pol1) + (unsigned)w * 2048u), [sl] "s"(bm + 7168u + (unsigned)w * 256u),
          [te] "s"(T_E), [ts] "s"(T_S), [d0] "v"(D[0]), [d1] "v"(D[1]), [d2] "v"(D[2]), [d3] "v"(D[3]),
          [fa] "v"(lb + L_FLAG + w * 4), [za] "v"(lb + L_MP + (pr + 1) * 512 + lane * 16), [st] "s"(s + 1)
        : "memory", "vcc", "scc", "s96", "s97", "v196", "v197", "v198", "v199",
          "v200", "v201", "v202", "v203", "v204", "v205", "v206", "v207", "v208", "v209", "v210", "v211", "v212", "v213", "v214", "v215",
          "v216", "v217", "v218", "v219", "v220", "v221", "v222", "v224", "v225", "v226", "v227", "v228", "v229", "v230", "v231",
          "v232", "v233", "v234", "v235", "v236", "v237", "v238", "v239", "v240", "v241", "v242", "v243", "v244", "v245", "v246", "v247", "v248", "v249");
      ST_AT(3);
      ST_END(s);
    }
  } else {
    unsigned cap = SPIN_CAP;
    Fetch F = {0u, 0u, 0u, 0ull};
    const unsigned gw = ctl_g(56 + ((rho * 28 + v) >> 3), pol0);
    auto fetch = [&](int must, unsigned lim) {
      for (int i = 0; i < 2 && F.go(R, gw, must, lim, cap); ++i) {
#pragma unroll
        for (int h = 0; h < 8; h += 4) dma<4, true>(R, lb + L_D + (F.nxt & 3u) * 16384u + ((w - 4) * 8 + h) * 1024, OFF_DX + (F.nxt * 448u + v * 16 + (w - 4) * 8 + h) * 1024u, lane * 16);
        F.issued(8);
      } };
    *(LAS unsigned*)(L + L_FLAG + (w - 4) * 4) = 0u;
    if (w < 6) fetch(0, 1);
    unsigned tb = 0;
    v4u hold[4] = {};
    auto put = [&](int s) {
#pragma unroll
      for (int i = 0; i < 4; ++i) st16c(R, OFF_MP + (unsigned)((s * 8 + 2 * i) * 28 + v) * 512u, (lane >> 5) * (28 * 512) + (lane & 31) * 16, hold[i]); };
    int est = 4 * T_M;
    __builtin_amdgcn_s_waitcnt(0x0F70);
    WG_BAR();
    PLACE_(AL_H);
    for (int s = 0;; ++s) {
      WG_BAR();
      const unsigned pa = pa_blk(s, pol0, pol1);
      if (w == 7) {
        tb = wall_fine();
        st4c(R, pa + v * 16, 0u, fixs(tb + (unsigned)((est >> 2) + T_P)));
        if (v == 0) st4c(R, CTL_P + s * 128, 0u, 1u); }
      else if (w < 6) fetch(-1, s + 4 < NSEG - 2 ? s + 4 : NSEG - 2);
      else if (s) put(s - 1);
      unsigned px;
      const v4u mp = mprime([&] {
        SB();
        asm volatile("s_cmp_lg_u32 %3, 6\n\ts_cbranch_scc1 1f\n\tbuffer_load_dword v248, %1, %2, 0 offen sc1\n1:" : "=&{v248}"(px) : "v"(lane < 32 ? pa + (unsigned)(lane < 28 ? lane : 0) * 16u : gw), "s"(R), "s"(w) : "scc", "memory");
        SB(); });
      if (s == NSEG - 1) { last(mp); break; }
      if (g < 2) *(LAS v4u*)(L + L_MP + pr * 512 + lane * 16) = mp;
      CFENCE();
      *(LAS unsigned*)(L + L_FLAG + (w - 4) * 4) = (unsigned)s + 1u;
      ST_AT(1);
      if (w < 6) {
        if (F.nxt <= (unsigned)s + 1u && s + 1 <= NSEG - 2) fetch(s + 1, s + 1);
        const unsigned n = F.since(s + 1);
        if (n >= 24u) WAIT_VM(24); else if (n >= 16u) WAIT_VM(16); else if (n >= 8u) WAIT_VM(8); else WAIT_VM(0);
      } else if (w == 6) {
        asm volatile("s_waitcnt vmcnt(0)" : "+{v248}"(px) :: "memory");
        *(LAS v2u*)(L + L_T) = pace_target(px, lane);
      }
      ST_AT(2);
      WG_BAR();
      if (w == 7) {
        f32x2 t[4];
#pragma unroll
        for (int i = 0; i < 4; ++i) t[i] = lds8f(L, L_RED + i * 512 + lane * 8);
        const f32x2 u = (t[0] + t[1]) + (t[2] + t[3]);
        st4c(R, ho_blk(s + 1, 0, pol0, pol1) + v * 256, lane * 4, fixs(pack2(u[0], u[1])));
        CFENCE();
        const int d = (int)(wall_fine() - tb), e = est >> 2;
        est += (d > e + 8 ? e + 8 : d) - e;
      } else if (w == 6) {
#pragma unroll
        for (int i = 0; i < 4; ++i) hold[i] = fixs4(lds16(L, L_MP + i * 1024 + lane * 16));
      } else F.seen(*(LAS unsigned*)(L + L_T + 4));
      ST_AT(3);
      ST_END(s);
    }
  }
  ST_DUMP();
}

DEV void role_q(rsrc_t R, LAS char* L, int h, int p, int w, int lane) {
  const int g = lane >> 4, rho = lane & 15, hd = rho >> 3, r = rho & 7;
  constexpr unsigned L_M = 0, L_RED = 7168, L_RED2 = 9216, L_X = 16384, L_DUMP = 49152;
  TUNE_DECL;
  const unsigned lb = (unsigned)(size_t)L;
  ST_DECL;
  if (w < 7) {
    int bud = POLL_BUDGET;
    v4u A[7][2];
#pragma unroll
    for (int t = 0; t < 7; ++t)
#pragma unroll
      for (int u = 0; u < 2; ++u) A[t][u] = ld16p(R, OFF_WF + (unsigned)((h * 28 + (t < 6 ? 6 * p + t : 24 + p)) * 14 + 2 * w + u) * 1024u, lane * 16);
    GET_POL();
    const unsigned mo = (unsigned)(((2 * w + hd) * 4 + g) * 128 + r * 16);
    const unsigned mb = lb + L_M + (unsigned)((3 * p * 4 + g) * 128 + r * 16), mh = lb + L_M + (unsigned)(((12 + (p >> 1)) * 4 + g) * 128 + r * 16 + 8 * (p & 1));
    const unsigned red = lb + (g < 2 ? L_RED + (unsigned)(w * 256 + r * 32 + g * 16) : L_DUMP + (unsigned)(w * 1024 + lane * 16));
    v4u k0, k1, k2, kh = {0u, 0u, 0u, 0u}, bh = {0u, 0u, 0u, 0u};
    __builtin_amdgcn_s_waitcnt(0x0F70);
    PLACE_(AL_Q);
    for (int s = 0; s < NSEG; ++s) {
      asm volatile(POLL_HEAD
        "buffer_load_dwordx4 v[232:235], %[mo], %[R], %[so] offen sc1\n\t" POLL_BUD("%[bud]")
        "s_waitcnt vmcnt(0)\n\t"
        MFMA "v[100:103], %[a00], v[232:235], 0\n\t"
        "v_max_u32 v128, v234, v235\n\tv_max3_u32 v128, v232, v233, v128\n\t"
        MFMA "v[104:107], %[a10], v[232:235], 0\n\t"
        "v_cmp_eq_u32 vcc, -1, v128\n\ts_cbranch_vccnz 0b\n3:\n\t"
        "ds_write_b128 %[mw], v[232:235]\n\t"
        "v_mov_b32_dpp v236, v232" ROR8 "v_mov_b32_dpp v237, v233" ROR8 "v_mov_b32_dpp v238, v234" ROR8 "v_mov_b32_dpp v239, v235" ROR8
        MFMA "v[108:111], %[a20], v[232:235], 0\n\t"
        MFMA "v[112:115], %[a30], v[232:235], 0\n\t"
        MFMA "v[100:103], %[a01], v[236:239], v[100:103]\n\t"
        MFMA "v[104:107], %[a11], v[236:239], v[104:107]\n\t"
        "s_waitcnt lgkmcnt(0)\n\ts_barrier\n\t"
        "ds_read_b128 v[128:131], %[mb]\n\tds_read_b128 v[132:135], %[mb] offset:512\n\tds_read_b64 v[156:157], %[mh]\n\tds_read_b128 v[136:139], %[mb] offset:1024\n\t"
        MFMA "v[108:111], %[a21], v[236:239], v[108:111]\n\t"
        MFMA "v[112:115], %[a31], v[236:239], v[112:115]\n\t"
        MFMA "v[116:119], %[a40], v[232:235], 0\n\t"
        CVT "v140, v100, v101\n\t" CVT "v141, v102, v103\n\t"
        MFMA "v[120:123], %[a50], v[232:235], 0\n\t"
        CVT "v142, v104, v105\n\t" CVT "v143, v106, v107\n\t"
        MFMA "v[116:119], %[a41], v[236:239], v[116:119]\n\t"
        CVT "v144, v108, v109\n\t" CVT "v145, v110, v111\n\t"
        MFMA "v[120:123], %[a51], v[236:239], v[120:123]\n\t"
        CVT "v146, v112, v113\n\t" CVT "v147, v114, v115\n\t"
        MFMA "v[124:127], %[a60], v[232:235], 0\n\t"
        "s_waitcnt lgkmcnt(3)\n\t"
        MFMA "v[100:103], v[140:143], v[128:131], 0\n\t"
        MFMA "v[124:127], %[a61], v[236:239], v[124:127]\n\t"
        CVT "v148, v116, v117\n\t" CVT "v149, v118, v119\n\t"
        "s_waitcnt lgkmcnt(0)\n\t"
        MFMA "v[100:103], v[144:147], v[132:135], v[100:103]\n\t"
        CVT "v150, v120, v121\n\t" CVT "v151, v122, v123\n\t"
        "s_nop 1\n\t"
        MFMA "v[100:103], v[148:151], v[136:139], v[100:103]\n\t"
        CVT "v152, v124, v125\n\t" CVT "v153, v126, v127\n\t"
        "s_nop 1\n\t"
        MFMA "v[100:103], v[152:155], v[156:159], v[100:103]\n\t"
        "s_nop 7\n\t"
        "ds_write_b128 %[red], v[100:103]\n\ts_waitcnt lgkmcnt(0)\n\ts_barrier"
        : [bud] "+s"(bud), "=&{v[140:143]}"(k0), "=&{v[144:147]}"(k1), "=&{v[148:151]}"(k2), "+{v[152:155]}"(kh), "+{v[156:159]}"(bh)
        : [mo] "v"(mo), [R] "s"(R), [so] "s"(ho_blk(s, 0, pol0, pol1)), [mw] "v"(lb + L_M + mo), [mb] "v"(mb), [mh] "v"(mh), [red] "v"(red),
          [a00] "v"(A[0][0]), [a01] "v"(A[0][1]), [a10] "v"(A[1][0]), [a11] "v"(A[1][1]), [a20] "v"(A[2][0]), [a21] "v"(A[2][1]), [a30] "v"(A[3][0]), [a31] "v"(A[3][1]),
          [a40] "v"(A[4][0]), [a41] "v"(A[4][1]), [a50] "v"(A[5][0]), [a51] "v"(A[5][1]), [a60] "v"(A[6][0]), [a61] "v"(A[6][1])
        : "memory", "vcc", "scc", "v100", "v101", "v102", "v103", "v104", "v105", "v106", "v107", "v108", "v109", "v110", "v111", "v112", "v113", "v114", "v115",
          "v116", "v117", "v118", "v119", "v120", "v121", "v122", "v123", "v124", "v125", "v126", "v127", "v128", "v129", "v130", "v131", "v132", "v133", "v134", "v135",
          "v136", "v137", "v138", "v139", "v232", "v233", "v234", "v235", "v236", "v237", "v238", "v239");
      ST_AT(2);
      const unsigned xb = L_X + (unsigned)(s & 3) * 8192u;
#pragma unroll
      for (int tt = 0; tt < 2; ++tt) {
        f32x4 sx = mma(k0, lds16(L, xb + tt * 4096 + lane * 16), (f32x4){0.f, 0.f, 0.f, 0.f});
        sx = mma(k1, lds16(L, xb + tt * 4096 + 1024 + lane * 16), sx);
        sx = mma(k2, lds16(L, xb + tt * 4096 + 2048 + lane * 16), sx);
        const v2u xh = lds8(L, xb + tt * 4096 + 3072 + lane * 16 + 8 * (p & 1));
        sx = mma(kh, (v4u){xh.x, xh.y, 0u, 0u}, sx);
        if (g < 2) *(LAS f32x4*)(L + L_RED2 + w * 1024 + (tt * 32 + rho * 2 + g) * 16) = sx;
      }
      if (w == 3 && s < NSEG - 1) pace_wall(pace_target(pace_load(R, pa_blk(s, pol0, pol1), lane & 31, 0u), lane).x - T_X);
      WG_BAR();
      ST_AT(3);
      ST_END(s);
    }
    BUD_DUMP();
  } else {
    auto fetch = [&](int n) {
      const int sg = n < NSEG ? n : NSEG - 1;
#pragma unroll
      for (int tt = 0; tt < 2; ++tt) {
        dma<3, false>(R, lb + L_X + (n & 3) * 8192 + tt * 4096, OFF_XF + (unsigned)((sg * 2 + tt) * 14 + 3 * p) * 1024u, lane * 16);
        dma<1, false>(R, lb + L_X + (n & 3) * 8192 + tt * 4096 + 3072, OFF_XF + (unsigned)((sg * 2 + tt) * 14 + 12 + (p >> 1)) * 1024u, lane * 16);
      } };
    fetch(0); fetch(1); fetch(2);
    GET_POL();
    WAIT_VM(0);
    __builtin_amdgcn_s_setprio(2);
    v2u sxm = {0u, 0u};
    auto put = [&](int s) { st8c(R, OFF_SXM + (unsigned)((s * 16 + h) * 8 + p) * 256u, (lane >> 5) * 1024 + (lane & 31) * 8, sxm); };
    PLACE_(AL_R);
    for (int s = 0; s < NSEG; ++s) {
      const unsigned so = ho_blk(s, 1, pol0, pol1) + (unsigned)((h >> 1) * 1024 + p * 256 + (h & 1) * 128);
      WG_BAR();
      if (s) put(s - 1);
      fetch(s + 3);
      ST_AT(2);
      WG_BAR();
      { f32x2 t[7];
#pragma unroll
        for (int i = 0; i < 7; ++i) t[i] = lds8f(L, L_RED + i * 256 + (lane & 31) * 8);
        const f32x2 u = ((t[0] + t[1]) + (t[2] + t[3])) + ((t[4] + t[5]) + t[6]);
        st4c(R, so, (lane & 31) * 4, fixs(pack2(u[0], u[1])));
        ST_AT(0); }
      WG_BAR();
      { f32x4 t[7];
#pragma unroll
        for (int i = 0; i < 7; ++i) t[i] = lds16f(L, L_RED2 + i * 1024 + lane * 16);
        const f32x4 u = ((t[0] + t[1]) + (t[2] + t[3])) + ((t[4] + t[5]) + t[6]);
        sxm = (v2u){fixs(pack2(u[0], u[1])), fixs(pack2(u[2], u[3]))}; }
      WAIT_VM(9);
      ST_AT(1);
      ST_END(s);
    }
    put(NSEG - 1);
  }
  ST_DUMP();
}

DEV void role_x(rsrc_t R, LAS char* L, int h, int w, int lane) {
  const int g = lane >> 4, rho = lane & 15, hd = rho >> 3, r = rho & 7;
  constexpr unsigned L_K = 7168, L_R = L_K + 4 * 28672;
  TUNE_DECL;
  GET_POL();
  const unsigned lb = (unsigned)(size_t)L;
  ST_DECL;
  if (w < 7) {
    int bud = POLL_BUDGET;
    const unsigned mo = (unsigned)(((2 * w + hd) * 4 + g) * 128 + r * 16);
    PLACE_(AL_X);
    for (int s = 0; s < NSEG - 1; ++s) {
      WG_BAR();
      v4u kx[2][2];
#pragma unroll
      for (int i = 0; i < 4; ++i) kx[i >> 1][i & 1] = lds16(L, L_K + (s & 3) * 28672 + ((i >> 1) * 14 + 2 * w + (i & 1)) * 1024 + lane * 16);
      const v4u f0 = poll16(R, ho_blk(s, 0, pol0, pol1), mo, bud);
      ST_AT(0);
      const v4u f1 = ror8v(f0);
      f32x4 a0 = mma(kx[0][0], f0, (f32x4){0.f, 0.f, 0.f, 0.f}), a1 = mma(kx[1][1], f0, (f32x4){0.f, 0.f, 0.f, 0.f});
      a0 = mma(kx[0][1], f1, a0); a1 = mma(kx[1][0], f1, a1);
      f32x4 o;
#pragma unroll
      for (int i = 0; i < 4; ++i) o[i] = hd ? a1[i] : a0[i];
      *(LAS f32x4*)(L + w * 1024 + (r * 8 + ((g * 2 + hd) ^ r)) * 16) = o;
      WG_BAR();
      ST_AT(2);
      ST_END(s);
      if (w == 0 && s < NSEG - 2) {
        __builtin_amdgcn_s_sleep(14);
        const v2u t = pace_target(pace_load(R, pa_blk(s, pol0, pol1), lane, gw_head(h, lane, pol0)), lane);
        *(LAS unsigned*)(L + L_R) = t.y;
        pace_wall(t.x);
      }
    }
    BUD_DUMP();
  } else {
    unsigned cap = SPIN_CAP;
    Fetch F = {0u, 0u, 0u, 0ull};
    auto fetch = [&](int must, unsigned lim) {
      for (int i = 0; i < 2 && F.go(R, gw_head(h, lane, pol0), must, lim, cap); ++i) {
#pragma unroll
        for (int c = 0; c < 7; ++c) dma<4, true>(R, lb + L_K + (F.nxt & 3u) * 28672u + c * 4096, OFF_KX + ((F.nxt * 16u + h) * 28u + 4 * c) * 1024u, lane * 16);
        F.issued(28);
      } };
    fetch(0, 1);
    const int kg = (lane & 7) ^ (lane >> 3);
    const unsigned eo = (unsigned)(((kg >> 1) * 8 + (lane >> 3)) * 16 + (kg & 1) * 8), lo = (unsigned)(lane >> 3) * 4u;
    PLACE_(AL_Y);
    for (int s = 0; s < NSEG - 1; ++s) {
      const unsigned n = F.since(s);
      if (n >= 60u) WAIT_VM(60); else if (n >= 28u) WAIT_VM(28); else WAIT_VM(0);
      WG_BAR();
      const unsigned rdy = *(LAS unsigned*)(L + L_R);
      ST_AT(1);
#define DPPX(op, d, ctl) "s_nop 1\n\t" op "_dpp " d ", " d ", " d " " ctl " row_mask:0xf bank_mask:0xf\n\t"
      asm volatile("s_waitcnt lgkmcnt(0)\n\ts_barrier\n\t"
        "ds_read_b128 v[100:103], %[la]\n\tds_read_b128 v[104:107], %[la] offset:1024\n\tds_read_b128 v[108:111], %[la] offset:2048\n\tds_read_b128 v[112:115], %[la] offset:3072\n\t"
        "ds_read_b128 v[116:119], %[la] offset:4096\n\tds_read_b128 v[120:123], %[la] offset:5120\n\tds_read_b128 v[124:127], %[la] offset:6144\n\t"
        "s_waitcnt lgkmcnt(5)\n\tv_pk_add_f32 v[100:101], v[100:101], v[104:105]\n\tv_pk_add_f32 v[102:103], v[102:103], v[106:107]\n\t"
        "s_waitcnt lgkmcnt(3)\n\tv_pk_add_f32 v[108:109], v[108:109], v[112:113]\n\tv_pk_add_f32 v[110:111], v[110:111], v[114:115]\n\t"
        "s_waitcnt lgkmcnt(1)\n\tv_pk_add_f32 v[116:117], v[116:117], v[120:121]\n\tv_pk_add_f32 v[118:119], v[118:119], v[122:123]\n\t"
        "v_pk_add_f32 v[100:101], v[100:101], v[108:109]\n\tv_pk_add_f32 v[102:103], v[102:103], v[110:111]\n\t"
        "s_waitcnt lgkmcnt(0)\n\tv_pk_add_f32 v[116:117], v[116:117], v[124:125]\n\tv_pk_add_f32 v[118:119], v[118:119], v[126:127]\n\t"
        "v_pk_add_f32 v[100:101], v[100:101], v[116:117]\n\tv_pk_add_f32 v[102:103], v[102:103], v[118:119]\n\t"
        "v_max_f32 v104, v100, v101\n\tv_max3_f32 v104, v102, v103, v104\n\t"
        DPPX("v_max_f32", "v104", "quad_perm:[1,0,3,2]") DPPX("v_max_f32", "v104", "quad_perm:[2,3,0,1]") DPPX("v_max_f32", "v104", "row_half_mirror")
        "v_sub_f32 v100, v100, v104\n\tv_sub_f32 v101, v101, v104\n\tv_sub_f32 v102, v102, v104\n\tv_sub_f32 v103, v103, v104\n\t"
        "v_exp_f32 v100, v100\n\tv_exp_f32 v101, v101\n\tv_exp_f32 v102, v102\n\tv_exp_f32 v103, v103\n\t"
        "v_add_f32 v105, v100, v101\n\tv_add_f32 v106, v102, v103\n\tv_add_f32 v105, v105, v106\n\t"
        DPPX("v_add_f32", "v105", "quad_perm:[1,0,3,2]") DPPX("v_add_f32", "v105", "quad_perm:[2,3,0,1]") DPPX("v_add_f32", "v105", "row_half_mirror")
        "v_rcp_f32 v106, v105\n\tv_log_f32 v105, v105\n\t"
        "v_mul_f32 v100, v100, v106\n\tv_mul_f32 v101, v101, v106\n\tv_mul_f32 v102, v102, v106\n\tv_mul_f32 v103, v103, v106\n\t"
        CVT "v100, v100, v101\n\t" CVT "v101, v102, v103\n\t"
        "v_add_f32 v104, v104, v105\n\t"
        "v_min_u32 v100, -2, v100\n\tv_min_u32 v101, -2, v101\n\tv_min_u32 v104, -2, v104\n\t"
        "buffer_store_dwordx2 v[100:101], %[eo], %[R], %[se] offen sc1\n\tbuffer_store_dword v104, %[lo], %[R], %[sl] offen sc1"
        :: [la] "v"(lb + lane * 16), [eo] "v"(eo), [lo] "v"(lo), [R] "s"(R), [se] "s"(ho_blk(s, 2, pol0, pol1) + (unsigned)h * 512u), [sl] "s"(ho_blk(s, 0, pol0, pol1) + 7168u + (unsigned)h * 64u)
        : "memory", "v100", "v101", "v102", "v103", "v104", "v105", "v106", "v107", "v108", "v109", "v110", "v111", "v112", "v113", "v114", "v115",
          "v116", "v117", "v118", "v119", "v120", "v121", "v122", "v123", "v124", "v125", "v126", "v127");
      ST_AT(0);
      F.T += 2;
      if (s) F.seen(rdy);
      fetch(s + 1, s + 3 < NSEG - 2 ? s + 3 : NSEG - 2);
      ST_END(s);
    }
  }
  ST_DUMP();
}

template <int TYPE>
DEV void role_g(rsrc_t R, const MegaArgs& a, LAS char* L, int gi, int w, int lane, unsigned& pol0) {
  const int wc = w & 3, ws = w >> 2, g = lane >> 4, rho = lane & 15, t0 = 8 * gi + 2 * wc;
  const unsigned lb = (unsigned)(size_t)L;
  v4u W[2][14];
  if (TYPE == 2) {
    const Tr T(lane, FF * 2);
    const rsrc_t RW = rsrc_of(ws ? a.wu : a.wg, DM * FF * 2);
#pragma unroll
    for (int t = 0; t < 14; ++t) T.load(RW, lb + ws * 57344 + wc * 14336, (unsigned)(wc * 112 * FF + 64 * (gi - 112)) * 2u, FF * 2, t);
    WAIT_VM(0);
    WG_BAR();
#pragma unroll
    for (int c = 0; c < 2; ++c)
#pragma unroll
      for (int k = 0; k < 14; ++k) W[c][k] = T.frag(L + c * 57344, k, wc);
    WG_BAR();
  }
  dma<4, false>(R, lb + w * 7168, OFF_XF + (unsigned)w * 7168u, lane * 16); dma<3, false>(R, lb + w * 7168 + 4096, OFF_XF + (unsigned)w * 7168u + 4096u, lane * 16);
  if (TYPE != 2) {
    const unsigned dw = lb + 57344 + (2 * wc + ws) * 13312, sw = OFF_WF + (unsigned)(t0 + ws) * 14336u;
#pragma unroll
    for (int k = 0; k < 12; k += 4) dma<4, false>(R, dw + k * 1024, sw + k * 1024, lane * 16);
    dma<1, false>(R, dw + 12288, sw + 12288u, lane * 16);
#pragma unroll
    for (int c = 0; c < 2; ++c) W[c][13] = ld16p(R, OFF_WF + (unsigned)((t0 + c) * 14 + 13) * 1024u, lane * 16);
  }
  ST_DECL;
  pol0 = get_pol(R, lane) & 8192u;
  WAIT_VM(0);
  ST_AT(0);
  if (TYPE != 2) {
    auto rdw = [&](int c) {
#pragma unroll
      for (int k = 0; k < 13; ++k) W[c][k] = lds16(L, 57344 + (2 * wc + c) * 13312 + k * 1024 + lane * 16); };
    if (ws) { rdw(1); WG_BAR(); rdw(0); } else { rdw(0); WG_BAR(); rdw(1); }
  }
  WG_BAR();
  for (int it = 0; it < 32; ++it) {
    LAS char* cur = L + (it & 1) * 57344 + ws * 28672 + lane * 16;
    const unsigned dst = lb + ((it + 1) & 1) * 57344 + w * 7168, src = OFF_XF + (unsigned)((it < 31 ? it + 1 : it) * 56 + w * 7) * 1024u;
    f32x4 acc[2][2];
#pragma unroll
    for (int c = 0; c < 2; ++c) acc[c][0] = acc[c][1] = (f32x4){0.f, 0.f, 0.f, 0.f};
    v4u x0[14], x1[14];
#pragma unroll
    for (int k = 0; k < 3; ++k) { x0[k] = *(LAS v4u*)(cur + k * 1024); x1[k] = *(LAS v4u*)(cur + 14336 + k * 1024); }
#pragma unroll
    for (int k = 0; k < 14; ++k) {
      if (k + 3 < 14) { x0[k + 3] = *(LAS v4u*)(cur + (k + 3) * 1024); x1[k + 3] = *(LAS v4u*)(cur + 14336 + (k + 3) * 1024); }
      SB();
#pragma unroll
      for (int c = 0; c < 2; ++c) {
        if (TYPE == 1) { acc[c][0] = mma(x0[k], W[c][k], acc[c][0]); acc[c][1] = mma(x1[k], W[c][k], acc[c][1]); }
        else { acc[c][0] = mma(W[c][k], x0[k], acc[c][0]); acc[c][1] = mma(W[c][k], x1[k], acc[c][1]); }
      }
      if (k < 7 && it) dma<1, false>(R, dst + k * 1024, src + k * 1024, lane * 16);
      SB();
    }
    const int s = 2 * it + ws;
    if (TYPE == 0) {
      const int h = t0 / 28, u = (t0 % 28) >> 1;
#pragma unroll
      for (int tt = 0; tt < 2; ++tt) st16c(R, OFF_KX + (unsigned)(((s * 16 + h) * 2 + tt) * 14 + u) * 1024u, lane * 16, pack8(acc[0][tt], acc[1][tt]));
    } else if (TYPE == 1) {
#pragma unroll
      for (int c = 0; c < 2; ++c) st16c(R, OFF_DX + (unsigned)(s * 448 + ((t0 - 448) % 28 + c) * 16 + (t0 - 448) / 28) * 1024u, lane * 16, pack8(acc[c][0], acc[c][1]));
    } else {
      const int F = (t0 - 896) >> 1;
      const f32x4 ha = acc[0][0] * acc[1][0], hb = acc[0][1] * acc[1][1];
      const auto r0 = __builtin_amdgcn_permlane16_swap(pack2(ha[0], ha[1]), pack2(hb[0], hb[1]), false, false);
      const auto r1 = __builtin_amdgcn_permlane16_swap(pack2(ha[2], ha[3]), pack2(hb[2], hb[3]), false, false);
      st16c(R, OFF_HF + (unsigned)(s * 112 + (F >> 1)) * 1024u + (unsigned)(F & 1) * 512u, (g & 1) * 57344 + ((g >> 1) * 16 + rho) * 16, (v4u){r0[0], r1[0], r0[1], r1[1]});
    }
    ST_AT(1);
    if (!it) { dma<4, false>(R, dst, src, lane * 16); dma<3, false>(R, dst + 4096, src + 4096u, lane * 16); }
    const bool all = it < 3 || it == 31;
    if (all) WAIT_VM(0); else if (TYPE == 2) WAIT_VM(1); else WAIT_VM(2);
    WG_BAR();
    ST_AT(3);
    ST_END(it);
    if (w == 0) st4c(R, ctl_g(gi, pol0), 0u, (unsigned)(all ? it + 1 : it));
  }
  if (w) ST_DUMP();
}

DEV void role_f1(rsrc_t R, char* wsb, LAS char* L, int task, int w, int lane, unsigned& cap, unsigned pol0) {
  const int s = task >> 1, h = (task & 1) * 8 + w, g = lane >> 4, rho = lane & 15;
  if (w < 7) dma<4, false>(R, (unsigned)(size_t)L + w * 4096, OFF_XF + (unsigned)(s * 28 + w * 4) * 1024u, lane * 16);
  wait_words(R, gw_head(h, lane, pol0), s >> 1, cap);
  v4u kx[28];
#pragma unroll
  for (int i = 0; i < 28; ++i) kx[i] = ld16c(R, OFF_KX + (unsigned)((s * 16 + h) * 28 + i) * 1024u, lane * 16);
  WAIT_VM(0);
  WG_BAR();
  f32x4 a00 = {0.f, 0.f, 0.f, 0.f}, a01 = a00, a11 = a00;
#pragma unroll
  for (int k = 0; k < 14; ++k) {
    const v4u x0 = lds16(L, k * 1024 + lane * 16), x1 = lds16(L, 14336 + k * 1024 + lane * 16);
    a00 = mma(kx[k], x0, a00); a01 = mma(kx[k], x1, a01); a11 = mma(kx[14 + k], x1, a11);
  }
  const float NEG = -3.0e38f;
#pragma unroll
  for (int i = 0; i < 4; ++i) { const bool ok = 4 * g + i <= rho; a00[i] = ok ? a00[i] : NEG; a11[i] = ok ? a11[i] : NEG; }
  const float m0 = x32max(x16max(fmaxf(fmaxf(a00[0], a00[1]), fmaxf(a00[2], a00[3]))));
  const float m1 = x32max(x16max(fmaxf(fmaxf(fmaxf(a01[0], a01[1]), fmaxf(a01[2], a01[3])), fmaxf(fmaxf(a11[0], a11[1]), fmaxf(a11[2], a11[3])))));
#pragma unroll
  for (int i = 0; i < 4; ++i) { a00[i] = ex2(a00[i] - m0); a01[i] = ex2(a01[i] - m1); a11[i] = ex2(a11[i] - m1); }
  const float l0 = x32add(x16add((a00[0] + a00[1]) + (a00[2] + a00[3])));
  const float l1 = x32add(x16add(((a01[0] + a01[1]) + (a01[2] + a01[3])) + ((a11[0] + a11[1]) + (a11[2] + a11[3]))));
  const float i0 = rcp(l0), i1 = rcp(l1);
  st16c(R, OFF_EX + (unsigned)((s * 16 + h) * 2) * 1024u, lane * 16, pack8(a00 * i0, (f32x4){0.f, 0.f, 0.f, 0.f}));
  st16c(R, OFF_EX + (unsigned)((s * 16 + h) * 2 + 1) * 1024u, lane * 16, pack8(a01 * i1, a11 * i1));
  if (g < 2) st4c(R, OFF_LX + (unsigned)(s * 16 + h) * 128u, lane * 4, __builtin_bit_cast(unsigned, g ? m1 + lg2(l1) : m0 + lg2(l0)));
  WAIT_VM(0);
  WG_BAR();
  if (w == 0 && lane == 0) signal(wsb, CTL_E + s * 128);
}

DEV v4u scale8(v4u e, float a) {
  v4u r;
#pragma unroll
  for (int q = 0; q < 4; ++q) r[q] = pack2(bflo(e[q]) * a, bfhi(e[q]) * a);
  return r;
}

DEV void role_f2(rsrc_t R, const MegaArgs& a, LAS char* L, int q, int grp, int w, int lane, unsigned& cap, unsigned pol0) {
  v4u Wd[7][4];
  { const Tr T(lane, DM * 2);
    const rsrc_t RW = rsrc_of(a.wd, DM * FF * 2);
#pragma unroll
    for (int r = 0; r < 2; ++r) {
#pragma unroll
      for (int t = 0; t < 16 - 4 * r; ++t) T.load(RW, (unsigned)(size_t)L + w * 16384, (unsigned)((224 * w + 128 * r) * DM + 64 * q) * 2u, DM * 2, t);
      WAIT_VM(0);
#pragma unroll
      for (int k = 0; k < 4 - r; ++k)
#pragma unroll
        for (int c = 0; c < 4; ++c) Wd[4 * r + k][c] = T.frag(L + w * 16384, k, c);
      asm volatile("s_waitcnt lgkmcnt(0)" ::: "memory");
    }
    WG_BAR(); }
  for (int s = grp; s < NSEG; s += 21) {
    asm volatile("" : "+v"(lane));
    wait_words(R, ctl_g((lane & 8) ? 56 + (((2 * w + (lane & 1)) * 28 + 4 * q) >> 3) : 112 + ((7 * w + ((lane & 7) < 7 ? (lane & 7) : 0)) >> 1), pol0), s >> 1, cap);
    f32x4 acc[4][2];
#pragma unroll
    for (int c = 0; c < 4; ++c) acc[c][0] = acc[c][1] = (f32x4){0.f, 0.f, 0.f, 0.f};
    { v4u hf[14];
#pragma unroll
      for (int i = 0; i < 14; ++i) hf[i] = ld16c(R, OFF_HF + (unsigned)((s * 2 + i / 7) * 56 + 7 * w + i % 7) * 1024u, lane * 16);
#pragma unroll
      for (int k = 0; k < 7; ++k)
#pragma unroll
        for (int c = 0; c < 4; ++c) { acc[c][0] = mma(Wd[k][c], hf[k], acc[c][0]); acc[c][1] = mma(Wd[k][c], hf[7 + k], acc[c][1]); } }
    wait_words(R, CTL_E + (unsigned)s * 128u, 1u, cap);
    v4u D[2][4], E[2][2]; float lse[2];
#pragma unroll
    for (int hh = 0; hh < 2; ++hh) {
      const unsigned sh = (unsigned)(s * 16 + 2 * w + hh);
#pragma unroll
      for (int c = 0; c < 4; ++c) D[hh][c] = ld16c(R, OFF_DX + (unsigned)(s * 448 + (4 * q + c) * 16 + 2 * w + hh) * 1024u, lane * 16);
      E[hh][0] = ld16c(R, OFF_EX + sh * 2048u, lane * 16); E[hh][1] = ld16c(R, OFF_EX + sh * 2048u + 1024u, lane * 16);
      lse[hh] = __builtin_bit_cast(float, ld4c(R, OFF_LX + sh * 128u, ((lane >> 5) * 16 + (lane & 15)) * 4));
    }
    const v2u xv = *(const GAS v2u*)(a.x + (size_t)(s * 32 + (w >> 2) * 16 + (lane & 15)) * DM + 64 * q + 16 * (w & 3) + 4 * (lane >> 4));
    v4u sx[4], mp[4];
    if (w == 0) {
      for (unsigned n = 0; n < cap; ++n) {
        if (__builtin_amdgcn_readfirstlane(ld4c(R, CTL_P + (unsigned)(s <= 60 ? s + 3 : s < NSEG - 1 ? s + 1 : s) * 128u, 0u))) break;
        __builtin_amdgcn_s_sleep(8);
        CFENCE();
      }
    }
    WG_BAR();
    for (unsigned n = 0;; ++n) {
      unsigned m = 0;
#pragma unroll
      for (int i = 0; i < 4; ++i) {
        sx[i] = ld16c(R, OFF_SXM + (unsigned)((s * 16 + 2 * w) * 2 + i) * 1024u, lane * 16);
        mp[i] = ld16c(R, OFF_MP + (unsigned)((s * 8 + w) * 28 + 4 * q + i) * 512u, (lane & 31) * 16);
      }
#pragma unroll
      for (int i = 0; i < 4; ++i) m = umax(m, umax(max4u(sx[i]), max4u(mp[i])));
      if (__all(m != SENT)) break;
      if (n >= cap) { cap = 0; break; }
      __builtin_amdgcn_s_sleep(1);
      CFENCE();
    }
    asm volatile("" : "+v"(lane));
    const int g = lane >> 4, rho = lane & 15;
    const v4u sel0 = onehot(0, rho < 8 ? rho : -1), sel1 = onehot(0, rho >= 8 ? rho - 8 : -1);
    v4u bm; float al[2][2];
#pragma unroll
    for (int hh = 0; hh < 2; ++hh) {
      f32x4 sv = mma(sel0, sx[2 * hh], (f32x4){0.f, 0.f, 0.f, 0.f});
      sv = mma(sel1, sx[2 * hh + 1], sv);
      const float mx = fmaxf(x16max(fmaxf(fmaxf(sv[0], sv[1]), fmaxf(sv[2], sv[3]))), lse[hh]);
      const float e0 = ex2(sv[0] - mx), e1 = ex2(sv[1] - mx), e2 = ex2(sv[2] - mx), e3 = ex2(sv[3] - mx), ea = ex2(lse[hh] - mx);
      const float inv = rcp(x16add((e0 + e1) + (e2 + e3)) + ea);
      bm[2 * hh] = pack2(e0 * inv, e1 * inv); bm[2 * hh + 1] = pack2(e2 * inv, e3 * inv);
      const unsigned au = __builtin_bit_cast(unsigned, ea * inv);
      const auto rr = __builtin_amdgcn_permlane32_swap(au, au, false, false);
      al[hh][0] = __builtin_bit_cast(float, (unsigned)rr[0]); al[hh][1] = __builtin_bit_cast(float, (unsigned)rr[1]);
    }
    const v4u zz = {0u, 0u, 0u, 0u};
    const v4u b0 = g < 2 ? bm : zz, b1 = g < 2 ? zz : bm;
#pragma unroll
    for (int c = 0; c < 4; ++c) { acc[c][0] = mma(mp[c], b0, acc[c][0]); acc[c][1] = mma(mp[c], b1, acc[c][1]); }
#pragma unroll
    for (int hh = 0; hh < 2; ++hh)
#pragma unroll
      for (int tt = 0; tt < 2; ++tt) {
        const v4u es = scale8(E[hh][tt], al[hh][tt]);
#pragma unroll
        for (int c = 0; c < 4; ++c) acc[c][tt] = mma(D[hh][c], es, acc[c][tt]);
      }
#pragma unroll
    for (int tt = 0; tt < 2; ++tt)
#pragma unroll
      for (int c = 0; c < 4; ++c) *(LAS f32x4*)(L + w * 8192 + (tt * 4 + c) * 1024 + lane * 16) = acc[c][tt];
    WG_BAR();
    f32x4 t[8];
#pragma unroll
    for (int i = 0; i < 8; ++i) t[i] = lds16f(L, i * 8192 + w * 1024 + lane * 16);
    const f32x4 u = ((t[0] + t[1]) + (t[2] + t[3])) + ((t[4] + t[5]) + (t[6] + t[7]));
    st8c(rsrc_of(a.out, S_ * DM * 2), (unsigned)((s * 32 + (w >> 2) * 16) * DM + 64 * q + 16 * (w & 3)) * 2u, (unsigned)(rho * DM + 4 * g) * 2u, (v2u){pack2(u[0] + bflo(xv.x), u[1] + bfhi(xv.x)), pack2(u[2] + bflo(xv.y), u[3] + bfhi(xv.y))});
    WG_BAR();
  }
}

__global__ void __launch_bounds__(512, 2) mega_kernel(MegaArgs a) {
  extern __shared__ __attribute__((aligned(16))) char smem[];
  LAS char* L = (LAS char*)smem;
  const int b = blockIdx.x, w = __builtin_amdgcn_readfirstlane(threadIdx.x >> 6), lane = threadIdx.x & 63;
  char* const wsb = ws_base(a.ws);
  const rsrc_t R = __builtin_amdgcn_make_buffer_rsrc(wsb, 0, WS_USED, 0x00020000);
  const int side = (b >> 2) & 1, idx = (b >> 3) * 4 + (b & 3);
  auto hold_off = [](unsigned d) { const unsigned t = wall() + d; while ((int)(wall() - t) < 0) __builtin_amdgcn_s_sleep(2); };
  TUNE_DECL;
  if (side == 0 && idx < 108) {
    hold_off(T_A);
    if (idx < 28) role_v(R, L, idx, w, lane);
    else if (idx < 44) role_x(R, L, idx - 28, w, lane);
    else role_q(R, L, (idx - 44) >> 2, (idx - 44) & 3, w, lane);
    return;
  }
  const int bi = side ? idx : 128 + idx - 108;
  ST_DECL;
  ST_AT(0);
  unsigned pol0;
  const int gi = 7 * (4 * ((bi >> 2) / 7) + (bi & 3)) + (bi >> 2) % 7;
  if (bi < 56) role_g<0>(R, a, L, gi, w, lane, pol0);
  else if (bi < 112) role_g<1>(R, a, L, gi, w, lane, pol0);
  else if (bi < 140) { hold_off(T_C); role_g<2>(R, a, L, bi, w, lane, pol0); }
  else pol0 = get_pol(R, lane) & 8192u;
  ST_AT(1);
  unsigned cap = SPIN_CAP;
  hold_off(bi * T_L);
  if (bi < 128) role_f1(R, wsb, L, bi, w, lane, cap, pol0);
  ST_AT(2);
  if (bi < 147) role_f2(R, a, L, bi < 140 ? (bi >> 2) % 7 : bi - 140, bi < 140 ? (bi & 3) + 4 * ((bi >> 2) / 7) : 20, w, lane, cap, pol0);
  ST_AT(3);
  ST_END(0);
  if (w == 0) ST_DUMP();
}

extern "C" void kernel_launch(void* const* d_in, const int* in_sizes, int n_in, void* d_out, int out_size, void* d_ws, size_t ws_size, hipStream_t stream) {
  ProArgs p;
  p.x = (const unsigned short*)d_in[0]; p.mem0 = (const unsigned short*)d_in[1];
  p.wq = (const unsigned short*)d_in[2]; p.wk = (const unsigned short*)d_in[3]; p.wv = (const unsigned short*)d_in[4]; p.wo = (const unsigned short*)d_in[5];
  p.ws = (char*)d_ws;
  (void)hipFuncSetAttribute((const void*)pro_kernel, hipFuncAttributeMaxDynamicSharedMemorySize, PRO_LDS);
  pro_kernel<<<PRO_GRID, 512, PRO_LDS, stream>>>(p);
  MegaArgs m; m.x = p.x;
  m.wg = (const unsigned short*)d_in[8]; m.wu = (const unsigned short*)d_in[9]; m.wd = (const unsigned short*)d_in[10]; m.out = (unsigned short*)d_out; m.ws = (char*)d_ws;
  (void)hipFuncSetAttribute((const void*)mega_kernel, hipFuncAttributeMaxDynamicSharedMemorySize, LDS_BYTES);
  mega_kernel<<<256, 512, LDS_BYTES, stream>>>(m);
}
```
